```python
import torch

def _softmax(x, axis=-1):
    return torch.softmax(x, dim=axis)
def _gelu(x):
    return torch.nn.functional.gelu(x, approximate="tanh")
def _silu(x):
    return torch.nn.functional.silu(x)
def _sigmoid(x):
    return torch.sigmoid(x)
def _layernorm(x, axis=-1, eps=1e-5):
    m = x.mean(dim=axis, keepdim=True)
    v = x.var(dim=axis, keepdim=True, unbiased=False)
    return (x - m) / torch.sqrt(v + eps)
def _scatter_add(tgt, idx, src, axis=0):
    return tgt.clone().index_add_(axis, idx, src)
def _segment_sum(src, seg, n):
    out = torch.zeros((n,) + src.shape[1:], dtype=src.dtype)
    return out.index_add_(0, seg, src)
def _pad(x, pw):
    flat = tuple(v for pair in reversed(pw) for v in pair)
    return torch.nn.functional.pad(x, flat)

kernel_name = 'concat_heads__gqa_block_cached__mha_block_cached__decode'

def setup_inputs():

    _w = (lambda a: (lambda t: t.to(torch.bfloat16) if t.is_floating_point() else t)(torch.as_tensor(a)))
    def _A_setup():
        B, Sq, D = 1, 1, 448
        Hq, Hkv, Sk, Dh = 16, 8, 32768, 256
        assert Hq % Hkv == 0, (Hq, Hkv)
        return {
            "x": _w((lambda *s: torch.randn(*s, dtype=torch.bfloat16) * 0.8)(B, Sq, D)),
            "k_cache": _w((lambda *s: torch.randn(*s, dtype=torch.bfloat16) * 1.6)(B, Hkv, Sk, Dh)),
            "v_cache": _w((lambda *s: torch.randn(*s, dtype=torch.bfloat16) * 0.2)(B, Hkv, Sk, Dh)),
            "wq": _w((lambda *s: torch.randn(*s, dtype=torch.bfloat16) * 1.6)(D, Hq * Dh) * (D ** -0.5)),
            "wo": _w((lambda *s: torch.randn(*s, dtype=torch.bfloat16) * 0.2)(Hq * Dh, D) * ((Hq * Dh) ** -0.5)),
        }
    _A_in = _A_setup()

    def _B_setup():
        B, Sq, D = 1, 1, 448
        H, Sk, Dh = 16, 32768, 256
        return {
            "x": _w((lambda *s: torch.randn(*s, dtype=torch.bfloat16) * 0.8)(B, Sq, D)),
            "k_cache": _w((lambda *s: torch.randn(*s, dtype=torch.bfloat16) * 1.6)(B, H, Sk, Dh)),
            "v_cache": _w((lambda *s: torch.randn(*s, dtype=torch.bfloat16) * 0.2)(B, H, Sk, Dh)),
            "wq": _w((lambda *s: torch.randn(*s, dtype=torch.bfloat16) * 1.6)(D, H * Dh) * (D ** -0.5)),
            "wo": _w((lambda *s: torch.randn(*s, dtype=torch.bfloat16) * 0.2)(H * Dh, D) * ((H * Dh) ** -0.5)),
        }
    _B_in = _B_setup()

    return {
        "A_x": _A_in["x"],
        "A_k_cache": _A_in["k_cache"],
        "A_v_cache": _A_in["v_cache"],
        "A_wq": _A_in["wq"],
        "A_wo": _A_in["wo"],
        "B_x": _B_in["x"],
        "B_k_cache": _B_in["k_cache"],
        "B_v_cache": _B_in["v_cache"],
        "B_wq": _B_in["wq"],
        "B_wo": _B_in["wo"],
    }

def reference(A_x, A_k_cache, A_v_cache, A_wq, A_wo, B_x, B_k_cache, B_v_cache, B_wq, B_wo):
    def _A_ref(x, k_cache, v_cache, wq, wo):
        B, Sq, D = x.shape
        Hq, Dh = 16, 256
        Hkv = k_cache.shape[1]
        rep = Hq // Hkv
        q = (lambda x, *d: x.permute(*d))((x @ wq).reshape(B, Sq, Hq, Dh), 0, 2, 1, 3)
        k = (lambda x, n, axis: torch.repeat_interleave(x, n, dim=axis))(k_cache, rep, axis=1)
        v = (lambda x, n, axis: torch.repeat_interleave(x, n, dim=axis))(v_cache, rep, axis=1)
        scores = torch.einsum("bhqd,bhkd->bhqk", q, k) * (Dh ** -0.5)
        p = _softmax(scores, axis=-1)
        o = torch.einsum("bhqk,bhkd->bhqd", p, v)
        return (lambda x, *d: x.permute(*d))(o, 0, 2, 1, 3).reshape(B, Sq, Hq * Dh) @ wo

    def _B_ref(x, k_cache, v_cache, wq, wo):
        B, Sq, D = x.shape
        H, Dh = 16, 256
        q = (lambda x, *d: x.permute(*d))((x @ wq).reshape(B, Sq, H, Dh), 0, 2, 1, 3)
        scores = torch.einsum("bhqd,bhkd->bhqk", q, k_cache) * (Dh ** -0.5)
        p = _softmax(scores, axis=-1)
        o = torch.einsum("bhqk,bhkd->bhqd", p, v_cache)
        return (lambda x, *d: x.permute(*d))(o, 0, 2, 1, 3).reshape(B, Sq, H * Dh) @ wo

    a = _A_ref(A_x, A_k_cache, A_v_cache, A_wq, A_wo)
    b = _B_ref(B_x, B_k_cache, B_v_cache, B_wq, B_wo)
    return torch.concatenate([a, b], axis=-1)
```

```cpp
#include <hip/hip_runtime.h>
#include <hip/hip_bf16.h>
#include <stdint.h>

#define GAS __attribute__((address_space(1)))
#define LAS __attribute__((address_space(3)))
typedef unsigned v4u __attribute__((ext_vector_type(4)));
typedef unsigned v2u __attribute__((ext_vector_type(2)));
typedef float f32x2 __attribute__((ext_vector_type(2)));
typedef float f32x4 __attribute__((ext_vector_type(4)));
typedef __bf16 bf16x2 __attribute__((ext_vector_type(2)));
typedef __amdgpu_buffer_rsrc_t rsrc_t;

namespace {
constexpr int NWG = 256, WAVES = 4, NTHR = WAVES * 64;
constexpr int SK = 32768, DH = 256, DM = 448, NQ = 4096;
constexpr int TK = 16;
constexpr int TILE_B = TK * DH * 2;
constexpr int NTA = (SK / 32) / TK / WAVES;
constexpr int NTB_ALL = (SK / 16) / TK / WAVES;
#define NSB 20
constexpr int POOL0 = NSB * 16 * WAVES;
constexpr int NPOOL = (NTB_ALL - NSB) * 16 * WAVES;
constexpr float SC = 0.0625f * 1.4426950408889634f;

constexpr size_t WS_CTR = 0;
constexpr size_t WS_Q = 8192;
constexpr size_t WS_ML = WS_Q + 16384;
constexpr size_t WS_MLB = WS_ML + 16 * 32 * 8;
constexpr size_t WS_PA = 65536;
constexpr size_t WS_PB = WS_PA + 16 * 32 * 1024;
constexpr size_t WS_Y = WS_PB + 16 * 16 * 1024;
constexpr size_t WS_END = WS_Y + 2 * 4 * 32 * 112 * 4;

struct Params {
    const unsigned short* x[2];
    const unsigned char* k[2];
    const unsigned char* v[2];
    const unsigned short* wq[2];
    const unsigned short* wo[2];
    unsigned short* out;
    unsigned char* ws;
};

__device__ __forceinline__ float dot2(unsigned a, unsigned b, float c) {
    return __builtin_amdgcn_fdot2_f32_bf16(__builtin_bit_cast(bf16x2, a), __builtin_bit_cast(bf16x2, b), c, false);
}
__device__ __forceinline__ unsigned cvtpk(float lo, float hi) {
    bf16x2 r = {(__bf16)lo, (__bf16)hi};
    return __builtin_bit_cast(unsigned, r);
}
template <int CTRL> __device__ __forceinline__ float dpp(float x) {
    return __builtin_bit_cast(float, __builtin_amdgcn_mov_dpp(__builtin_bit_cast(int, x), CTRL, 0xf, 0xf, true));
}
constexpr int XOR1 = 0xB1, XOR2 = 0x4E, HMIRROR = 0x141, ROR8 = 0x128;
__device__ __forceinline__ float bflo(unsigned u) { return __builtin_bit_cast(float, u << 16); }
__device__ __forceinline__ float bfhi(unsigned u) { return __builtin_bit_cast(float, u & 0xffff0000u); }
__device__ __forceinline__ float asf(unsigned u) { return __builtin_bit_cast(float, u); }
__device__ __forceinline__ unsigned asu(float f) { return __builtin_bit_cast(unsigned, f); }
__device__ __forceinline__ float ex2(float x) { return __builtin_amdgcn_exp2f(x); }
__device__ __forceinline__ v4u ldnt(rsrc_t r, int voff, unsigned soff) { return __builtin_amdgcn_raw_buffer_load_b128(r, voff, soff, 2); }
__device__ __forceinline__ float swap32_add(float a, float b) {
    auto r = __builtin_amdgcn_permlane32_swap(__builtin_bit_cast(unsigned, a), __builtin_bit_cast(unsigned, b), false, false);
    return asf(r[0]) + asf(r[1]);
}
__device__ __forceinline__ float swap16_add(float a, float b) {
    auto r = __builtin_amdgcn_permlane16_swap(__builtin_bit_cast(unsigned, a), __builtin_bit_cast(unsigned, b), false, false);
    return asf(r[0]) + asf(r[1]);
}
__device__ __forceinline__ float xmax_groups(float x) {
    x = fmaxf(x, dpp<ROR8>(x));
    auto s = __builtin_amdgcn_permlane16_swap(__builtin_bit_cast(unsigned, x), __builtin_bit_cast(unsigned, x), false, false);
    x = fmaxf(asf(s[0]), asf(s[1]));
    auto t = __builtin_amdgcn_permlane32_swap(__builtin_bit_cast(unsigned, x), __builtin_bit_cast(unsigned, x), false, false);
    return fmaxf(asf(t[0]), asf(t[1]));
}
__device__ __forceinline__ float xsum_groups(float x) {
    x = x + dpp<ROR8>(x);
    auto s = __builtin_amdgcn_permlane16_swap(__builtin_bit_cast(unsigned, x), __builtin_bit_cast(unsigned, x), false, false);
    x = asf(s[0]) + asf(s[1]);
    auto t = __builtin_amdgcn_permlane32_swap(__builtin_bit_cast(unsigned, x), __builtin_bit_cast(unsigned, x), false, false);
    return asf(t[0]) + asf(t[1]);
}
#define PIN(x) asm volatile("" : "+v"(x)::"memory")
#define SB() __builtin_amdgcn_sched_barrier(0)

__global__ void __launch_bounds__(NTHR) k_qproj(Params p) {
    __shared__ float red[WAVES][32];
    const int b = blockIdx.x, t = threadIdx.x, lane = t & 63, w = t >> 6;
    if (b == 0 && t < 16) ((unsigned*)(p.ws + WS_CTR))[t * 64] = 0u;
    const int cb = (b & 7) * 32 + (b >> 3);
    const int pr = cb >> 7, col0 = (cb & 127) * 32;
    const unsigned short* wq = p.wq[pr];
    const unsigned short* x = p.x[pr];
    const int rg = t >> 2, ch = t & 3;
    float acc[8];
#pragma unroll
    for (int e = 0; e < 8; ++e) acc[e] = 0.f;
    v4u wv[7]; unsigned short xv[7];
#pragma unroll
    for (int i = 0; i < 7; ++i) {
        const int r = rg + 64 * i;
        wv[i] = *(const v4u*)(wq + (size_t)r * NQ + col0 + ch * 8);
        xv[i] = x[r];
    }
#pragma unroll
    for (int i = 0; i < 7; ++i) {
        const float xf = __builtin_bit_cast(float, (unsigned)xv[i] << 16);
#pragma unroll
        for (int e = 0; e < 4; ++e) { acc[2 * e] = fmaf(xf, bflo(wv[i][e]), acc[2 * e]); acc[2 * e + 1] = fmaf(xf, bfhi(wv[i][e]), acc[2 * e + 1]); }
    }
#pragma unroll
    for (int e = 0; e < 8; ++e) {
        float v = acc[e];
        v += __shfl_xor(v, 4); v += __shfl_xor(v, 8); v += __shfl_xor(v, 16); v += __shfl_xor(v, 32);
        acc[e] = v;
    }
    if (lane < 4) {
#pragma unroll
        for (int e = 0; e < 8; ++e) red[w][lane * 8 + e] = acc[e];
    }
    __syncthreads();
    if (t < 32) {
        float v = 0.f;
#pragma unroll
        for (int ww = 0; ww < WAVES; ++ww) v += red[ww][t];
        ((unsigned short*)(p.ws + WS_Q))[pr * NQ + col0 + t] = (unsigned short)(cvtpk(v, 0.f) & 0xffffu);
    }
}

template <int G> struct State { float m[G], l[G]; f32x2 acc[G][16]; };

template <int G>
__device__ __forceinline__ void qk_piece(const v4u& kv, const v4u (&q)[G][4], int jj, float (&x)[G]) {
#pragma unroll
    for (int g = 0; g < G; ++g) {
        float a = x[g];
#pragma unroll
        for (int i = 0; i < 4; ++i) a = dot2(kv[i], q[g][jj][i], a);
        x[g] = a;
    }
}
template <int G>
__device__ __forceinline__ void softmax_step(float (&x0)[G], float (&x1)[G], State<G>& s, float (&p0)[G], float (&p1)[G]) {
#pragma unroll
    for (int g = 0; g < G; ++g) {
        float a = x0[g], b = x1[g];
        a += dpp<XOR1>(a); b += dpp<XOR1>(b);
        a += dpp<XOR2>(a); b += dpp<XOR2>(b);
        a += dpp<HMIRROR>(a); b += dpp<HMIRROR>(b);
        const unsigned pk = cvtpk(a, b);
        const float sa = bflo(pk) * SC, sb = bfhi(pk) * SC;
        const float mn = fmaxf(s.m[g], fmaxf(sa, sb));
        const float al = ex2(s.m[g] - mn);
        p0[g] = ex2(sa - mn); p1[g] = ex2(sb - mn);
        s.l[g] = fmaf(s.l[g], al, p0[g] + p1[g]);
        s.m[g] = mn;
#pragma unroll
        for (int i = 0; i < 16; ++i) s.acc[g][i] *= al;
    }
}
template <int G>
__device__ __forceinline__ void pv_piece(const v4u& vv, const float (&p)[G], int jj, State<G>& s) {
#pragma unroll
    for (int i = 0; i < 4; ++i) {
        const f32x2 vf = {bflo(vv[i]), bfhi(vv[i])};
#pragma unroll
        for (int g = 0; g < G; ++g) { const f32x2 pp = {p[g], p[g]}; s.acc[g][jj * 4 + i] = __builtin_elementwise_fma(vf, pp, s.acc[g][jj * 4 + i]); }
    }
}
template <int G>
__device__ __forceinline__ void flush_state(State<G>& s, LAS float* rec0, int rec_stride, int lane) {
#pragma unroll
    for (int g = 0; g < G; ++g) {
        const float M = xmax_groups(s.m[g]);
        const float wt = ex2(s.m[g] - M);
        const float L = xsum_groups(s.l[g] * wt);
        float v[32];
#pragma unroll
        for (int i = 0; i < 16; ++i) { v[2 * i] = s.acc[g][i][0] * wt; v[2 * i + 1] = s.acc[g][i][1] * wt; }
        float a[16], b8[8], c4[4];
#pragma unroll
        for (int i = 0; i < 16; ++i) a[i] = swap32_add(v[i], v[i + 16]);
#pragma unroll
        for (int i = 0; i < 8; ++i) b8[i] = swap16_add(a[i], a[i + 8]);
        const bool hi8 = (lane & 8) != 0;
#pragma unroll
        for (int i = 0; i < 4; ++i) { const float keep = hi8 ? b8[i + 4] : b8[i], send = hi8 ? b8[i] : b8[i + 4]; c4[i] = keep + dpp<ROR8>(send); }
        LAS float* rec = rec0 + g * rec_stride;
        const int d0 = 64 * (lane >> 4) + 8 * (lane & 7) + 4 * ((lane >> 3) & 1);
        *(LAS f32x4*)(rec + d0) = f32x4{c4[0], c4[1], c4[2], c4[3]};
        if (lane == 0) { rec[256] = M; rec[257] = L; }
    }
}

constexpr int REC = 260;

template <int G>
__device__ __forceinline__ void state_init(State<G>& s) {
#pragma unroll
    for (int g = 0; g < G; ++g) { s.m[g] = -1e30f; s.l[g] = 0.f;
#pragma unroll
        for (int i = 0; i < 16; ++i) s.acc[g][i] = f32x2{0.f, 0.f}; }
}

__global__ void __launch_bounds__(NTHR) k_attn(Params p) {
    __shared__ __attribute__((aligned(16))) float lds[WAVES * 3 * REC];
    const int b = blockIdx.x, t = threadIdx.x, lane = t & 63, w = __builtin_amdgcn_readfirstlane(t >> 6);
    const int gA = b >> 5, iA = b & 31, hB = b >> 4, jB = b & 15;
    const rsrc_t rkA = __builtin_amdgcn_make_buffer_rsrc((void*)p.k[0], 0, 8u * SK * DH * 2u, 0x00020000);
    const rsrc_t rvA = __builtin_amdgcn_make_buffer_rsrc((void*)p.v[0], 0, 8u * SK * DH * 2u, 0x00020000);
    const rsrc_t rkB = __builtin_amdgcn_make_buffer_rsrc((void*)p.k[1], 0, 16u * SK * DH * 2u, 0x00020000);
    const rsrc_t rvB = __builtin_amdgcn_make_buffer_rsrc((void*)p.v[1], 0, 16u * SK * DH * 2u, 0x00020000);
    const int vo = (lane >> 3) * 512 + (lane & 7) * 16;
    auto pc = [&](rsrc_t r, unsigned o, int j) -> v4u { return ldnt(r, vo + (j & 3) * 128, o + (j >> 2) * 4096); };
    const unsigned baseA = (unsigned)gA * (SK * 512u) + (unsigned)iA * (NTA * WAVES * TILE_B) + (unsigned)w * TILE_B;
    const unsigned headB = (unsigned)hB * (SK * 512u);
    const unsigned baseB = headB + (unsigned)jB * (NSB * WAVES * TILE_B) + (unsigned)w * TILE_B;
    v4u R[8];
#pragma unroll
    for (int j = 0; j < 8; ++j) R[j] = pc(rkA, baseA, j);
    SB();
    GAS unsigned* ctr = (GAS unsigned*)(p.ws + WS_CTR) + hB * 64;
    asm volatile("" : "+v"(ctr));
    const unsigned short* qws = (const unsigned short*)(p.ws + WS_Q);
    v4u qA[2][4], qB[1][4];
#pragma unroll
    for (int jj = 0; jj < 4; ++jj) {
        const int d = 8 * ((lane & 7) + 8 * jj);
        qA[0][jj] = *(const v4u*)(qws + (2 * gA) * DH + d);
        qA[1][jj] = *(const v4u*)(qws + (2 * gA + 1) * DH + d);
        qB[0][jj] = *(const v4u*)(qws + NQ + hB * DH + d);
    }
    LAS float* L = (LAS float*)lds;
    {
        State<2> s; state_init(s);
        unsigned oc = baseA;
#pragma unroll 1
        for (int i = 0; i < NTA; ++i) {
            float x0[2] = {0.f, 0.f}, x1[2] = {0.f, 0.f};
#pragma unroll
            for (int j = 0; j < 8; ++j) {
                if (j < 4) { qk_piece<2>(R[j], qA, j & 3, x0); PIN(x0[0]); PIN(x0[1]); } else { qk_piece<2>(R[j], qA, j & 3, x1); PIN(x1[0]); PIN(x1[1]); }
                R[j] = pc(rvA, oc, j);
                SB();
            }
            float p0[2], p1[2];
            softmax_step<2>(x0, x1, s, p0, p1);
            const bool last = (i == NTA - 1);
            const rsrc_t rkn = last ? rkB : rkA;
            const unsigned on = last ? baseB : oc + WAVES * TILE_B;
            SB();
#pragma unroll
            for (int j = 0; j < 8; ++j) {
                if (j < 4) pv_piece<2>(R[j], p0, j & 3, s); else pv_piece<2>(R[j], p1, j & 3, s);
                PIN(s.acc[0][(j & 3) * 4 + 3]); PIN(s.acc[1][(j & 3) * 4 + 3]);
                R[j] = pc(rkn, on, j);
                SB();
            }
            oc = on;
        }
        flush_state<2>(s, L + (w * 3) * REC, REC, lane);
    }
    {
        State<1> s; state_init(s);
        unsigned oc = baseB;
        int n = 1;
        while (true) {
            unsigned tk = 0xffffffffu;
            if (NPOOL > 0 && n >= NSB) { if (lane == 0) tk = __hip_atomic_fetch_add(ctr, 1u, __ATOMIC_RELAXED, __HIP_MEMORY_SCOPE_AGENT); }
            SB();
            float x0[1] = {0.f}, x1[1] = {0.f};
#pragma unroll
            for (int j = 0; j < 8; ++j) {
                if (j < 4) { qk_piece<1>(R[j], qB, j & 3, x0); PIN(x0[0]); } else { qk_piece<1>(R[j], qB, j & 3, x1); PIN(x1[0]); }
                R[j] = pc(rvB, oc, j);
                SB();
            }
            float p0[1], p1[1];
            softmax_step<1>(x0, x1, s, p0, p1);
            unsigned on; bool have = true;
            if (n < NSB) on = oc + WAVES * TILE_B;
            else { const unsigned tt = __builtin_amdgcn_readfirstlane(tk); have = tt < (unsigned)NPOOL; on = headB + (POOL0 + tt) * TILE_B; }
            SB();
            if (!have) {
#pragma unroll
                for (int j = 0; j < 8; ++j) { if (j < 4) pv_piece<1>(R[j], p0, j & 3, s); else pv_piece<1>(R[j], p1, j & 3, s); }
                break;
            }
#pragma unroll
            for (int j = 0; j < 8; ++j) {
                if (j < 4) pv_piece<1>(R[j], p0, j & 3, s); else pv_piece<1>(R[j], p1, j & 3, s);
                PIN(s.acc[0][(j & 3) * 4 + 3]);
                R[j] = pc(rkB, on, j);
                SB();
            }
            oc = on; ++n;
        }
        flush_state<1>(s, L + (w * 3 + 2) * REC, REC, lane);
    }
    __syncthreads();
    if (t < 192) {
        const int hs = t >> 6, d0 = (t & 63) * 4;
        float M = -1e30f;
#pragma unroll
        for (int ww = 0; ww < WAVES; ++ww) M = fmaxf(M, L[(ww * 3 + hs) * REC + 256]);
        f32x4 a = {0.f, 0.f, 0.f, 0.f}; float Ls = 0.f;
#pragma unroll
        for (int ww = 0; ww < WAVES; ++ww) {
            const LAS float* rec = L + (ww * 3 + hs) * REC;
            const float wt = ex2(rec[256] - M);
            a += *(const LAS f32x4*)(rec + d0) * wt; Ls = fmaf(rec[257], wt, Ls);
        }
        float* dst; float* ml;
        if (hs < 2) { const int hq = 2 * gA + hs; dst = (float*)(p.ws + WS_PA) + ((size_t)hq * 32 + iA) * 256; ml = (float*)(p.ws + WS_ML) + (hq * 32 + iA) * 2; }
        else { dst = (float*)(p.ws + WS_PB) + ((size_t)hB * 16 + jB) * 256; ml = (float*)(p.ws + WS_MLB) + (hB * 16 + jB) * 2; }
        *(f32x4*)(dst + d0) = a;
        if ((t & 63) == 0) { ml[0] = M; ml[1] = Ls; }
    }
}

__global__ void __launch_bounds__(NTHR) k_combine(Params p) {
    __shared__ __attribute__((aligned(16))) float red[8][128];
    __shared__ float ov[128];
    __shared__ float yred[WAVES][128];
    const int b = blockIdx.x, t = threadIdx.x, lane = t & 63, w = t >> 6;
    const int pr = b >> 7, hq = (b >> 3) & 15, hf = (b >> 2) & 1, cb = b & 3;
    const int S = pr == 0 ? 32 : 16;
    const int rg = t >> 4, cc = t & 15;
    v4u wv[8];
    const unsigned short* wo = p.wo[pr] + (size_t)(hq * 256 + hf * 128) * DM + cb * 112;
#pragma unroll
    for (int i = 0; i < 8; ++i) wv[i] = cc < 14 ? *(const v4u*)(wo + (size_t)(rg + 16 * i) * DM + cc * 8) : v4u{0, 0, 0, 0};
    const float* ml = (const float*)(p.ws + (pr == 0 ? WS_ML : WS_MLB)) + hq * S * 2;
    const float* pp = (const float*)(p.ws + (pr == 0 ? WS_PA : WS_PB)) + (size_t)hq * S * 256 + hf * 128;
    float ms = -1e30f, ls = 0.f;
    if (lane < S) { ms = ml[2 * lane]; ls = ml[2 * lane + 1]; }
    float M = ms;
#pragma unroll
    for (int o = 1; o < 64; o <<= 1) M = fmaxf(M, __shfl_xor(M, o));
    const float wt = lane < S ? ex2(ms - M) : 0.f;
    float Lt = wt * ls;
#pragma unroll
    for (int o = 1; o < 64; o <<= 1) Lt += __shfl_xor(Lt, o);
    const int c = t & 31, sg = t >> 5;
    f32x4 a = {0.f, 0.f, 0.f, 0.f};
#pragma unroll
    for (int i = 0; i < 4; ++i) {
        const int s = sg + 8 * i;
        const float ws_ = __shfl(wt, s);
        if (s < S) a += *(const f32x4*)(pp + (size_t)s * 256 + c * 4) * ws_;
    }
    *(f32x4*)(&red[sg][c * 4]) = a;
    __syncthreads();
    if (t < 128) {
        float v = 0.f;
#pragma unroll
        for (int g = 0; g < 8; ++g) v += red[g][t];
        v = v / Lt;
        ov[t] = bflo(cvtpk(v, 0.f));
    }
    __syncthreads();
    float y[8];
#pragma unroll
    for (int e = 0; e < 8; ++e) y[e] = 0.f;
#pragma unroll
    for (int i = 0; i < 8; ++i) {
        const float o = ov[rg + 16 * i];
#pragma unroll
        for (int e = 0; e < 4; ++e) { y[2 * e] = fmaf(o, bflo(wv[i][e]), y[2 * e]); y[2 * e + 1] = fmaf(o, bfhi(wv[i][e]), y[2 * e + 1]); }
    }
#pragma unroll
    for (int e = 0; e < 8; ++e) { float v = y[e]; v += __shfl_xor(v, 16); v += __shfl_xor(v, 32); y[e] = v; }
    if (lane < 14) {
#pragma unroll
        for (int e = 0; e < 8; ++e) yred[w][lane * 8 + e] = y[e];
    }
    __syncthreads();
    if (t < 112) {
        float v = 0.f;
#pragma unroll
        for (int ww = 0; ww < WAVES; ++ww) v += yred[ww][t];
        ((float*)(p.ws + WS_Y))[((size_t)(pr * 4 + cb) * 32 + hq * 2 + hf) * 112 + t] = v;
    }
}

__global__ void __launch_bounds__(128) k_final(Params p) {
    const int b = blockIdx.x, t = threadIdx.x;
    if (t < 112) {
        const float* y = (const float*)(p.ws + WS_Y) + (size_t)b * 32 * 112 + t;
        float v = 0.f;
#pragma unroll
        for (int s = 0; s < 32; ++s) v += y[s * 112];
        p.out[(b >> 2) * DM + (b & 3) * 112 + t] = (unsigned short)(cvtpk(v, 0.f) & 0xffffu);
    }
}
}

extern "C" void kernel_launch(void* const* d_in, const int* in_sizes, int n_in, void* d_out, int out_size, void* d_ws, size_t ws_size,
                              hipStream_t stream) {
    Params p;
    p.x[0] = (const unsigned short*)d_in[0]; p.k[0] = (const unsigned char*)d_in[1]; p.v[0] = (const unsigned char*)d_in[2];
    p.wq[0] = (const unsigned short*)d_in[3]; p.wo[0] = (const unsigned short*)d_in[4];
    p.x[1] = (const unsigned short*)d_in[5]; p.k[1] = (const unsigned char*)d_in[6]; p.v[1] = (const unsigned char*)d_in[7];
    p.wq[1] = (const unsigned short*)d_in[8]; p.wo[1] = (const unsigned short*)d_in[9];
    p.out = (unsigned short*)d_out; p.ws = (unsigned char*)d_ws;
    k_qproj<<<NWG, NTHR, 0, stream>>>(p);
    k_attn<<<NWG, NTHR, 0, stream>>>(p);
    k_combine<<<NWG, NTHR, 0, stream>>>(p);
    k_final<<<8, 128, 0, stream>>>(p);
}
```

```cpp
#include <hip/hip_runtime.h>
#include <hip/hip_bf16.h>
#include <stdint.h>

#define GAS __attribute__((address_space(1)))
#define LAS __attribute__((address_space(3)))
typedef unsigned v4u __attribute__((ext_vector_type(4)));
typedef unsigned v2u __attribute__((ext_vector_type(2)));
typedef float f32x2 __attribute__((ext_vector_type(2)));
typedef float f32x4 __attribute__((ext_vector_type(4)));
typedef __bf16 bf16x2 __attribute__((ext_vector_type(2)));
typedef __amdgpu_buffer_rsrc_t rsrc_t;
typedef unsigned long long u64;

namespace {
constexpr int NWG = 256, WAVES = 4, NTHR = WAVES * 64;
constexpr int SK = 32768, DH = 256, DM = 448, NQ = 4096;
constexpr int TK = 16;
constexpr int TILE_B = TK * DH * 2;
constexpr int NTA = (SK / 32) / TK / WAVES;
constexpr int NTB_ALL = (SK / 16) / TK / WAVES;
#define NSB 20
constexpr int POOL0 = NSB * 16 * WAVES;
constexpr int NPOOL = (NTB_ALL - NSB) * 16 * WAVES;
constexpr float SC = 0.0625f * 1.4426950408889634f;
constexpr unsigned INVALID = 0xffffffffu;
constexpr unsigned SPIN_CAP = 1u << 18;

constexpr unsigned WS_EPOCH = 0;
constexpr unsigned WS_READY = 4096;
constexpr unsigned WS_CTR = 8192;
constexpr unsigned WS_QG = 16384;
constexpr unsigned WS_ML = WS_QG + 65536;
constexpr unsigned WS_MLB = WS_ML + 16 * 32 * 16;
constexpr unsigned WS_PA = 131072;
constexpr unsigned WS_PB = WS_PA + 16 * 32 * 1024;
constexpr unsigned WS_Y = WS_PB + 16 * 16 * 1024;
constexpr unsigned WS_DIAG = WS_Y + 2 * 4 * 32 * 112 * 4;
constexpr unsigned WS_END = WS_DIAG + 256 * 16 * 8;

struct Params {
    const unsigned short* x[2];
    const unsigned char* k[2];
    const unsigned char* v[2];
    const unsigned short* wq[2];
    const unsigned short* wo[2];
    unsigned short* out;
    unsigned char* ws;
};

__device__ __forceinline__ float asf(unsigned u) { return __builtin_bit_cast(float, u); }
__device__ __forceinline__ unsigned asu(float f) { return __builtin_bit_cast(unsigned, f); }
__device__ __forceinline__ float dot2(unsigned a, unsigned b, float c) {
    return __builtin_amdgcn_fdot2_f32_bf16(__builtin_bit_cast(bf16x2, a), __builtin_bit_cast(bf16x2, b), c, false);
}
__device__ __forceinline__ unsigned cvtpk(float lo, float hi) {
    bf16x2 r = {(__bf16)lo, (__bf16)hi};
    return __builtin_bit_cast(unsigned, r);
}
template <int CTRL> __device__ __forceinline__ float dpp(float x) {
    return asf((unsigned)__builtin_amdgcn_mov_dpp((int)asu(x), CTRL, 0xf, 0xf, true));
}
constexpr int XOR1 = 0xB1, XOR2 = 0x4E, HMIRROR = 0x141, ROR8 = 0x128;
__device__ __forceinline__ float bflo(unsigned u) { return asf(u << 16); }
__device__ __forceinline__ float bfhi(unsigned u) { return asf(u & 0xffff0000u); }
__device__ __forceinline__ float ex2(float x) { return __builtin_amdgcn_exp2f(x); }
constexpr int AUX_NT = 2, AUX_SC1 = 16;
__device__ __forceinline__ v4u ldnt(rsrc_t r, int voff, unsigned soff) { return __builtin_amdgcn_raw_buffer_load_b128(r, voff, soff, AUX_NT); }
__device__ __forceinline__ v4u ld128c(rsrc_t r, int voff) { return __builtin_amdgcn_raw_buffer_load_b128(r, voff, 0, AUX_SC1); }
__device__ __forceinline__ v2u ld64c(rsrc_t r, int voff) { return __builtin_amdgcn_raw_buffer_load_b64(r, voff, 0, AUX_SC1); }
__device__ __forceinline__ void st128c(v4u v, rsrc_t r, int voff) { __builtin_amdgcn_raw_buffer_store_b128(v, r, voff, 0, AUX_SC1); }
__device__ __forceinline__ void st64c(v2u v, rsrc_t r, int voff) { __builtin_amdgcn_raw_buffer_store_b64(v, r, voff, 0, AUX_SC1); }
__device__ __forceinline__ void st32c(unsigned v, rsrc_t r, int voff) { __builtin_amdgcn_raw_buffer_store_b32(v, r, voff, 0, AUX_SC1); }
__device__ __forceinline__ float swap32_add(float a, float b) {
    auto r = __builtin_amdgcn_permlane32_swap(asu(a), asu(b), false, false);
    return asf(r[0]) + asf(r[1]);
}
__device__ __forceinline__ float swap16_add(float a, float b) {
    auto r = __builtin_amdgcn_permlane16_swap(asu(a), asu(b), false, false);
    return asf(r[0]) + asf(r[1]);
}
__device__ __forceinline__ float x16_max(float x) { auto s = __builtin_amdgcn_permlane16_swap(asu(x), asu(x), false, false); return fmaxf(asf(s[0]), asf(s[1])); }
__device__ __forceinline__ float x32_max(float x) { auto s = __builtin_amdgcn_permlane32_swap(asu(x), asu(x), false, false); return fmaxf(asf(s[0]), asf(s[1])); }
__device__ __forceinline__ float x16_sum(float x) { auto s = __builtin_amdgcn_permlane16_swap(asu(x), asu(x), false, false); return asf(s[0]) + asf(s[1]); }
__device__ __forceinline__ float x32_sum(float x) { auto s = __builtin_amdgcn_permlane32_swap(asu(x), asu(x), false, false); return asf(s[0]) + asf(s[1]); }
__device__ __forceinline__ float xmax_groups(float x) { x = fmaxf(x, dpp<ROR8>(x)); return x32_max(x16_max(x)); }
__device__ __forceinline__ float xsum_groups(float x) { x = x + dpp<ROR8>(x); return x32_sum(x16_sum(x)); }
__device__ __forceinline__ float wave_max(float x) { x = fmaxf(x, dpp<XOR1>(x)); x = fmaxf(x, dpp<XOR2>(x)); x = fmaxf(x, dpp<HMIRROR>(x)); return xmax_groups(x); }
__device__ __forceinline__ float wave_sum(float x) { x += dpp<XOR1>(x); x += dpp<XOR2>(x); x += dpp<HMIRROR>(x); return xsum_groups(x); }
__device__ __forceinline__ bool all_lanes(bool ok) { return __builtin_amdgcn_ballot_w64(ok) == __builtin_amdgcn_ballot_w64(true); }
__device__ __forceinline__ bool valid4(v4u v) { return v.x != INVALID && v.y != INVALID && v.z != INVALID && v.w != INVALID; }
#define PIN(x) asm volatile("" : "+v"(x)::"memory")
#define SB() __builtin_amdgcn_sched_barrier(0)
#define BARRIER() do { asm volatile("s_waitcnt lgkmcnt(0)" ::: "memory"); __builtin_amdgcn_s_barrier(); asm volatile("" ::: "memory"); } while (0)
#define STAMP(i) do { } while (0)

template <int G> struct State { float m[G], l[G]; f32x2 acc[G][16]; };

template <int G>
__device__ __forceinline__ void qk_piece(const v4u& kv, const v4u (&q)[G][4], int jj, float (&x)[G]) {
#pragma unroll
    for (int g = 0; g < G; ++g) {
        float a = x[g];
#pragma unroll
        for (int i = 0; i < 4; ++i) a = dot2(kv[i], q[g][jj][i], a);
        x[g] = a;
    }
}
template <int G>
__device__ __forceinline__ void softmax_step(float (&x0)[G], float (&x1)[G], State<G>& s, float (&p0)[G], float (&p1)[G]) {
#pragma unroll
    for (int g = 0; g < G; ++g) {
        float a = x0[g], b = x1[g];
        a += dpp<XOR1>(a); b += dpp<XOR1>(b);
        a += dpp<XOR2>(a); b += dpp<XOR2>(b);
        a += dpp<HMIRROR>(a); b += dpp<HMIRROR>(b);
        const unsigned pk = cvtpk(a, b);
        const float sa = bflo(pk) * SC, sb = bfhi(pk) * SC;
        const float mn = fmaxf(s.m[g], fmaxf(sa, sb));
        const float al = ex2(s.m[g] - mn);
        p0[g] = ex2(sa - mn); p1[g] = ex2(sb - mn);
        s.l[g] = fmaf(s.l[g], al, p0[g] + p1[g]);
        s.m[g] = mn;
#pragma unroll
        for (int i = 0; i < 16; ++i) s.acc[g][i] *= al;
    }
}
template <int G>
__device__ __forceinline__ void pv_piece(const v4u& vv, const float (&p)[G], int jj, State<G>& s) {
#pragma unroll
    for (int i = 0; i < 4; ++i) {
        const f32x2 vf = {bflo(vv[i]), bfhi(vv[i])};
#pragma unroll
        for (int g = 0; g < G; ++g) { const f32x2 pp = {p[g], p[g]}; s.acc[g][jj * 4 + i] = __builtin_elementwise_fma(vf, pp, s.acc[g][jj * 4 + i]); }
    }
}
constexpr int REC = 260;
template <int G>
__device__ __forceinline__ void flush_state(State<G>& s, LAS float* rec0, int lane) {
#pragma unroll
    for (int g = 0; g < G; ++g) {
        const float M = xmax_groups(s.m[g]);
        const float wt = ex2(s.m[g] - M);
        const float L = xsum_groups(s.l[g] * wt);
        float v[32];
#pragma unroll
        for (int i = 0; i < 16; ++i) { v[2 * i] = s.acc[g][i][0] * wt; v[2 * i + 1] = s.acc[g][i][1] * wt; }
        float a[16], b8[8], c4[4];
#pragma unroll
        for (int i = 0; i < 16; ++i) a[i] = swap32_add(v[i], v[i + 16]);
#pragma unroll
        for (int i = 0; i < 8; ++i) b8[i] = swap16_add(a[i], a[i + 8]);
        const bool hi8 = (lane & 8) != 0;
#pragma unroll
        for (int i = 0; i < 4; ++i) { const float keep = hi8 ? b8[i + 4] : b8[i], send = hi8 ? b8[i] : b8[i + 4]; c4[i] = keep + dpp<ROR8>(send); }
        LAS float* rec = rec0 + g * REC;
        const int d0 = 64 * (lane >> 4) + 8 * (lane & 7) + 4 * ((lane >> 3) & 1);
        *(LAS f32x4*)(rec + d0) = f32x4{c4[0], c4[1], c4[2], c4[3]};
        if (lane == 0) { rec[256] = M; rec[257] = L; }
    }
}
template <int G>
__device__ __forceinline__ void state_init(State<G>& s) {
#pragma unroll
    for (int g = 0; g < G; ++g) { s.m[g] = -1e30f; s.l[g] = 0.f;
#pragma unroll
        for (int i = 0; i < 16; ++i) s.acc[g][i] = f32x2{0.f, 0.f}; }
}

constexpr int L_REC = 0;
constexpr int L_RED = WAVES * 3 * REC;
constexpr int L_OV = L_RED + 8 * 128;
constexpr int L_Q = L_OV + 128;
constexpr int L_END = L_Q + 384;

__global__ void __launch_bounds__(NTHR, 2) k_fused(Params p) {
    __shared__ __attribute__((aligned(16))) float lds[L_END];
    LAS float* L = (LAS float*)lds;
    const int b = blockIdx.x, t = threadIdx.x, lane = t & 63, w = __builtin_amdgcn_readfirstlane(t >> 6);
    const int gA = b >> 5, iA = b & 31, hB = b >> 4, jB = b & 15;
    const int it = (b & 7) * 32 + (b >> 3);
    const rsrc_t rws = __builtin_amdgcn_make_buffer_rsrc((void*)p.ws, 0, WS_END, 0x00020000);
    STAMP(0);
    const v2u ep = ld64c(rws, WS_EPOCH);
    {
        int off = -1; unsigned val = INVALID;
        if (t < 64) off = WS_PA + ((2 * gA) * 32 + iA) * 1024 + t * 16;
        else if (t < 128) off = WS_PA + ((2 * gA + 1) * 32 + iA) * 1024 + (t - 64) * 16;
        else if (t < 192) off = WS_PB + (hB * 16 + jB) * 1024 + (t - 128) * 16;
        else if (t < 220) off = WS_Y + ((((it >> 7) * 4 + (it & 3)) * 32 + ((it >> 2) & 31)) * 112) * 4 + (t - 192) * 16;
        else if (t < 222) off = WS_ML + ((2 * gA + (t - 220)) * 32 + iA) * 16;
        else if (t == 222) off = WS_MLB + (hB * 16 + jB) * 16;
        else if (t == 223 && b < 16) { off = WS_CTR + b * 256; val = 0u; }
        if (off >= 0) st128c(v4u{val, val, val, val}, rws, off);
    }
    SB();
    const int qpr = it >> 7, qcol0 = (it & 127) * 32;
    const int rg = t >> 2, ch = t & 3;
    v4u wv[7]; unsigned short xv[7];
    {
        const unsigned short* wq = p.wq[qpr] + qcol0 + ch * 8;
        const unsigned short* x = p.x[qpr];
#pragma unroll
        for (int i = 0; i < 7; ++i) { const int r = rg + 64 * i; wv[i] = *(const GAS v4u*)(wq + (size_t)r * NQ); xv[i] = *(const GAS unsigned short*)(x + r); }
    }
    SB();
    const int cpr = it >> 7, chq = (it >> 3) & 15, chf = (it >> 2) & 1, ccb = it & 3;
    const int org = t >> 4, occ = t & 15;
    v4u wo[8];
    {
        const unsigned short* wop = p.wo[cpr] + (size_t)(chq * 256 + chf * 128 + org) * DM + ccb * 112 + (occ < 14 ? occ : 13) * 8;
#pragma unroll
        for (int i = 0; i < 8; ++i) wo[i] = *(const GAS v4u*)(wop + (size_t)(16 * i) * DM);
    }
    SB();
    const rsrc_t rkA = __builtin_amdgcn_make_buffer_rsrc((void*)p.k[0], 0, 8u * SK * DH * 2u, 0x00020000);
    const rsrc_t rvA = __builtin_amdgcn_make_buffer_rsrc((void*)p.v[0], 0, 8u * SK * DH * 2u, 0x00020000);
    const rsrc_t rkB = __builtin_amdgcn_make_buffer_rsrc((void*)p.k[1], 0, 16u * SK * DH * 2u, 0x00020000);
    const rsrc_t rvB = __builtin_amdgcn_make_buffer_rsrc((void*)p.v[1], 0, 16u * SK * DH * 2u, 0x00020000);
    const int vo = (lane >> 3) * 512 + (lane & 7) * 16;
    auto pc = [&](rsrc_t r, unsigned o, int j) -> v4u { return ldnt(r, vo + (j & 3) * 128, o + (j >> 2) * 4096); };
    const unsigned baseA = (unsigned)gA * (SK * 512u) + (unsigned)iA * (NTA * WAVES * TILE_B) + (unsigned)w * TILE_B;
    const unsigned headB = (unsigned)hB * (SK * 512u);
    const unsigned baseB = headB + (unsigned)jB * (NSB * WAVES * TILE_B) + (unsigned)w * TILE_B;
    v4u R[8];
#pragma unroll
    for (int j = 0; j < 8; ++j) R[j] = pc(rkA, baseA, j);
    SB();
    {
        float acc[8];
#pragma unroll
        for (int e = 0; e < 8; ++e) acc[e] = 0.f;
#pragma unroll
        for (int i = 0; i < 7; ++i) {
            const float xf = asf((unsigned)xv[i] << 16);
#pragma unroll
            for (int e = 0; e < 4; ++e) { acc[2 * e] = fmaf(xf, bflo(wv[i][e]), acc[2 * e]); acc[2 * e + 1] = fmaf(xf, bfhi(wv[i][e]), acc[2 * e + 1]); }
        }
#pragma unroll
        for (int e = 0; e < 8; ++e) { float v = acc[e]; v += dpp<0x124>(v);   v += dpp<ROR8>(v); v = x32_sum(x16_sum(v)); acc[e] = v; }
        if (lane < 4) {
            *(LAS f32x4*)(L + L_RED + w * 32 + lane * 8) = f32x4{acc[0], acc[1], acc[2], acc[3]};
            *(LAS f32x4*)(L + L_RED + w * 32 + lane * 8 + 4) = f32x4{acc[4], acc[5], acc[6], acc[7]};
        }
    }
    asm volatile("s_waitcnt vmcnt(16)" ::: "memory");
    BARRIER();
    const u64 E = ((u64)ep.y << 32) | ep.x;
    const u64 tag = (E + 1) & 0xffffffffffffull;
    if (t < 32) {
        const float v = (L[L_RED + t] + L[L_RED + 32 + t]) + (L[L_RED + 64 + t] + L[L_RED + 96 + t]);
        const u64 g = (tag << 16) | (cvtpk(v, 0.f) & 0xffffu);
        st64c(v2u{(unsigned)g, (unsigned)(g >> 32)}, rws, WS_QG + (qpr * NQ + qcol0 + t) * 8);
    } else if (t == 32) {
        st64c(v2u{(unsigned)tag, (unsigned)(tag >> 32)}, rws, WS_READY + b * 8);
    }
    STAMP(1);
    {
        const unsigned tlo = (unsigned)tag, thi = (unsigned)(tag >> 32);
        const unsigned glo = tlo << 16, ghi = (unsigned)(tag >> 16);
        v2u g0, g1, g2;
        for (unsigned spins = 0;; ++spins) {
            const v2u rd = ld64c(rws, WS_READY + t * 8);
            g0 = ld64c(rws, WS_QG + ((2 * gA) * DH + t) * 8);
            g1 = ld64c(rws, WS_QG + ((2 * gA + 1) * DH + t) * 8);
            g2 = ld64c(rws, WS_QG + (NQ + hB * DH + t) * 8);
            const bool ok = rd.x == tlo && rd.y == thi && (g0.x & 0xffff0000u) == glo && g0.y == ghi && (g1.x & 0xffff0000u) == glo && g1.y == ghi &&
                            (g2.x & 0xffff0000u) == glo && g2.y == ghi;
            if (all_lanes(ok) || spins > SPIN_CAP) break;
            __builtin_amdgcn_s_sleep(2);
        }
        LAS unsigned short* qs = (LAS unsigned short*)(L + L_Q);
        qs[t] = (unsigned short)g0.x; qs[256 + t] = (unsigned short)g1.x; qs[512 + t] = (unsigned short)g2.x;
    }
    BARRIER();
    if (b == 0 && t == 0) { const u64 e1 = E + 1; st64c(v2u{(unsigned)e1, (unsigned)(e1 >> 32)}, rws, WS_EPOCH); }
    v4u qA[2][4], qB[1][4];
    {
        const LAS unsigned char* qs = (const LAS unsigned char*)(L + L_Q);
#pragma unroll
        for (int jj = 0; jj < 4; ++jj) {
            const int d = 16 * ((lane & 7) + 8 * jj);
            qA[0][jj] = *(const LAS v4u*)(qs + d); qA[1][jj] = *(const LAS v4u*)(qs + 512 + d); qB[0][jj] = *(const LAS v4u*)(qs + 1024 + d);
        }
    }
    GAS unsigned* ctr = (GAS unsigned*)(p.ws + WS_CTR) + hB * 64;
    asm volatile("" : "+v"(ctr));
    STAMP(2);
    {
        State<2> s; state_init(s);
        unsigned oc = baseA;
#pragma unroll 1
        for (int i = 0; i < NTA; ++i) {
            float x0[2] = {0.f, 0.f}, x1[2] = {0.f, 0.f};
#pragma unroll
            for (int j = 0; j < 8; ++j) {
                if (j < 4) { qk_piece<2>(R[j], qA, j & 3, x0); PIN(x0[0]); PIN(x0[1]); } else { qk_piece<2>(R[j], qA, j & 3, x1); PIN(x1[0]); PIN(x1[1]); }
                R[j] = pc(rvA, oc, j);
                SB();
            }
            float p0[2], p1[2];
            softmax_step<2>(x0, x1, s, p0, p1);
            const bool last = (i == NTA - 1);
            const rsrc_t rkn = last ? rkB : rkA;
            const unsigned on = last ? baseB : oc + WAVES * TILE_B;
            SB();
#pragma unroll
            for (int j = 0; j < 8; ++j) {
                if (j < 4) pv_piece<2>(R[j], p0, j & 3, s); else pv_piece<2>(R[j], p1, j & 3, s);
                PIN(s.acc[0][(j & 3) * 4 + 3]); PIN(s.acc[1][(j & 3) * 4 + 3]);
                R[j] = pc(rkn, on, j);
                SB();
            }
            oc = on;
        }
        flush_state<2>(s, L + L_REC + (w * 3) * REC, lane);
    }
    STAMP(3);
    {
        State<1> s; state_init(s);
        unsigned oc = baseB;
        int n = 1;
        while (true) {
            unsigned tk = 0xffffffffu;
            if (NPOOL > 0 && n >= NSB) { if (lane == 0) tk = __hip_atomic_fetch_add(ctr, 1u, __ATOMIC_RELAXED, __HIP_MEMORY_SCOPE_AGENT); }
            SB();
            float x0[1] = {0.f}, x1[1] = {0.f};
#pragma unroll
            for (int j = 0; j < 8; ++j) {
                if (j < 4) { qk_piece<1>(R[j], qB, j & 3, x0); PIN(x0[0]); } else { qk_piece<1>(R[j], qB, j & 3, x1); PIN(x1[0]); }
                R[j] = pc(rvB, oc, j);
                SB();
            }
            float p0[1], p1[1];
            softmax_step<1>(x0, x1, s, p0, p1);
            unsigned on; bool have = true;
            if (n < NSB) on = oc + WAVES * TILE_B;
            else { const unsigned tt = __builtin_amdgcn_readfirstlane(tk); have = tt < (unsigned)NPOOL; on = headB + (POOL0 + tt) * TILE_B; }
            SB();
            if (!have) {
#pragma unroll
                for (int j = 0; j < 8; ++j) { if (j < 4) pv_piece<1>(R[j], p0, j & 3, s); else pv_piece<1>(R[j], p1, j & 3, s); }
                break;
            }
#pragma unroll
            for (int j = 0; j < 8; ++j) {
                if (j < 4) pv_piece<1>(R[j], p0, j & 3, s); else pv_piece<1>(R[j], p1, j & 3, s);
                PIN(s.acc[0][(j & 3) * 4 + 3]);
                R[j] = pc(rkB, on, j);
                SB();
            }
            oc = on; ++n;
        }
        flush_state<1>(s, L + L_REC + (w * 3 + 2) * REC, lane);
    }
    STAMP(8 + w);
    BARRIER();
    STAMP(4);
    if (t < 192) {
        const int hs = w, d0 = lane * 4;
        float M = -1e30f;
#pragma unroll
        for (int ww = 0; ww < WAVES; ++ww) M = fmaxf(M, L[L_REC + (ww * 3 + hs) * REC + 256]);
        f32x4 a = {0.f, 0.f, 0.f, 0.f}; float Ls = 0.f;
#pragma unroll
        for (int ww = 0; ww < WAVES; ++ww) {
            const LAS float* rec = L + L_REC + (ww * 3 + hs) * REC;
            const float wt = ex2(rec[256] - M);
            a += *(const LAS f32x4*)(rec + d0) * wt; Ls = fmaf(rec[257], wt, Ls);
        }
        int dst, ml;
        if (hs < 2) { const int hq = 2 * gA + hs; dst = WS_PA + (hq * 32 + iA) * 1024; ml = WS_ML + (hq * 32 + iA) * 16; }
        else { dst = WS_PB + (hB * 16 + jB) * 1024; ml = WS_MLB + (hB * 16 + jB) * 16; }
        st128c(__builtin_bit_cast(v4u, a), rws, dst + lane * 16);
        if (lane == 0) st64c(v2u{asu(M), asu(Ls)}, rws, ml);
    }
    const int S = cpr == 0 ? 32 : 16;
    float wt, Lt;
    {
        const int mlb = (cpr == 0 ? WS_ML : WS_MLB) + chq * S * 16;
        const int sl = lane < S ? lane : 0;
        v2u ml;
        for (unsigned spins = 0;; ++spins) {
            ml = ld64c(rws, mlb + sl * 16);
            if (all_lanes(ml.x != INVALID && ml.y != INVALID) || spins > SPIN_CAP) break;
            __builtin_amdgcn_s_sleep(2);
        }
        const float ms = lane < S ? asf(ml.x) : -1e30f;
        const float M = wave_max(ms);
        wt = lane < S ? ex2(ms - M) : 0.f;
        Lt = wave_sum(wt * asf(ml.y));
    }
    STAMP(5);
    {
        const int c = t & 31, sg = t >> 5;
        const int pb = (cpr == 0 ? WS_PA : WS_PB) + chq * S * 1024 + chf * 512 + c * 16;
        v4u d[4];
        const int ni = S >> 3;
        for (unsigned spins = 0;; ++spins) {
            bool ok = true;
#pragma unroll
            for (int i = 0; i < 4; ++i) { const int s = sg + 8 * (i < ni ? i : 0); d[i] = ld128c(rws, pb + s * 1024); }
#pragma unroll
            for (int i = 0; i < 4; ++i) ok = ok && valid4(d[i]);
            if (all_lanes(ok) || spins > SPIN_CAP) break;
            __builtin_amdgcn_s_sleep(1);
        }
        f32x4 a = {0.f, 0.f, 0.f, 0.f};
#pragma unroll
        for (int i = 0; i < 4; ++i) {
            const float ws_ = i < ni ? __shfl(wt, sg + 8 * i) : 0.f;
            a += __builtin_bit_cast(f32x4, d[i]) * ws_;
        }
        *(LAS f32x4*)(L + L_RED + sg * 128 + c * 4) = a;
    }
    BARRIER();
    if (t < 128) {
        float v = 0.f;
#pragma unroll
        for (int g = 0; g < 8; ++g) v += L[L_RED + g * 128 + t];
        L[L_OV + t] = bflo(cvtpk(v / Lt, 0.f));
    }
    BARRIER();
    {
        float y[8];
#pragma unroll
        for (int e = 0; e < 8; ++e) y[e] = 0.f;
#pragma unroll
        for (int i = 0; i < 8; ++i) {
            const float o = L[L_OV + org + 16 * i];
#pragma unroll
            for (int e = 0; e < 4; ++e) { y[2 * e] = fmaf(o, bflo(wo[i][e]), y[2 * e]); y[2 * e + 1] = fmaf(o, bfhi(wo[i][e]), y[2 * e + 1]); }
        }
#pragma unroll
        for (int e = 0; e < 8; ++e) y[e] = x32_sum(x16_sum(y[e]));
        if (lane < 14) {
            *(LAS f32x4*)(L + L_RED + w * 128 + lane * 8) = f32x4{y[0], y[1], y[2], y[3]};
            *(LAS f32x4*)(L + L_RED + w * 128 + lane * 8 + 4) = f32x4{y[4], y[5], y[6], y[7]};
        }
    }
    BARRIER();
    if (t < 28) {
        const f32x4 v = (*(const LAS f32x4*)(L + L_RED + t * 4) + *(const LAS f32x4*)(L + L_RED + 128 + t * 4)) +
                        (*(const LAS f32x4*)(L + L_RED + 256 + t * 4) + *(const LAS f32x4*)(L + L_RED + 384 + t * 4));
        st128c(__builtin_bit_cast(v4u, v), rws, WS_Y + (((cpr * 4 + ccb) * 32 + chq * 2 + chf) * 112) * 4 + t * 16);
    }
    STAMP(6);
    if ((it & 7) == 0 && t < 224) {
        const int r = it >> 3, fpr = r >> 4, fcb = (r >> 2) & 3, sub = r & 3;
        const int c = t >> 5, s = t & 31;
        const int off = WS_Y + (((fpr * 4 + fcb) * 32 + s) * 112 + sub * 28 + c * 4) * 4;
        v4u d;
        for (unsigned spins = 0;; ++spins) {
            d = ld128c(rws, off);
            if (all_lanes(valid4(d)) || spins > SPIN_CAP) break;
            __builtin_amdgcn_s_sleep(1);
        }
        float v[4] = {asf(d.x), asf(d.y), asf(d.z), asf(d.w)};
#pragma unroll
        for (int e = 0; e < 4; ++e) { float z = v[e]; z += dpp<XOR1>(z); z += dpp<XOR2>(z); z += dpp<HMIRROR>(z); z += dpp<ROR8>(z); v[e] = x16_sum(z); }
        if (s == 0) *(GAS v2u*)(p.out + fpr * DM + fcb * 112 + sub * 28 + c * 4) = v2u{cvtpk(v[0], v[1]), cvtpk(v[2], v[3])};
    }
    STAMP(7);
}
}

extern "C" void kernel_launch(void* const* d_in, const int* in_sizes, int n_in, void* d_out, int out_size, void* d_ws, size_t ws_size,
                              hipStream_t stream) {
    Params p;
    p.x[0] = (const unsigned short*)d_in[0]; p.k[0] = (const unsigned char*)d_in[1]; p.v[0] = (const unsigned char*)d_in[2];
    p.wq[0] = (const unsigned short*)d_in[3]; p.wo[0] = (const unsigned short*)d_in[4];
    p.x[1] = (const unsigned short*)d_in[5]; p.k[1] = (const unsigned char*)d_in[6]; p.v[1] = (const unsigned char*)d_in[7];
    p.wq[1] = (const unsigned short*)d_in[8]; p.wo[1] = (const unsigned short*)d_in[9];
    p.out = (unsigned short*)d_out; p.ws = (unsigned char*)d_ws;
    k_fused<<<NWG, NTHR, 0, stream>>>(p);
}
```

```cpp
#define PREF 1
#define NSB 23
#include <hip/hip_runtime.h>
#include <hip/hip_bf16.h>
#include <stdint.h>

#define GAS __attribute__((address_space(1)))
#define LAS __attribute__((address_space(3)))
typedef unsigned v4u __attribute__((ext_vector_type(4)));
typedef unsigned v2u __attribute__((ext_vector_type(2)));
typedef float f32x2 __attribute__((ext_vector_type(2)));
typedef float f32x4 __attribute__((ext_vector_type(4)));
typedef __bf16 bf16x2 __attribute__((ext_vector_type(2)));
typedef __amdgpu_buffer_rsrc_t rsrc_t;
typedef unsigned long long u64;

namespace {
constexpr int NWG = 256, WAVES = 4, NTHR = WAVES * 64;
constexpr int SK = 32768, DH = 256, DM = 448, NQ = 4096;
constexpr int TK = 16;
constexpr int TILE_B = TK * DH * 2;
constexpr int NTA = (SK / 32) / TK / WAVES;
constexpr int NTB_ALL = (SK / 16) / TK / WAVES;
#define LB 2
#define WQ_AUX 2
constexpr int POOL0 = NSB * 16 * WAVES;
constexpr int NPOOL = (NTB_ALL - NSB) * 16 * WAVES;
constexpr float SC = 0.0625f * 1.4426950408889634f;
constexpr unsigned INVALID = 0xffffffffu;
constexpr unsigned SPIN_CAP = 1u << 18;

constexpr unsigned WS_EPOCH = 0;
constexpr unsigned WS_READY = 4096;
constexpr unsigned WS_CTR = 8192;
constexpr unsigned WS_QG = 16384;
constexpr unsigned WS_ML = WS_QG + 65536;
constexpr unsigned WS_MLB = WS_ML + 16 * 32 * 16;
constexpr unsigned WS_PA = 131072;
constexpr unsigned WS_PB = WS_PA + 16 * 32 * 1024;
constexpr unsigned WS_Y = WS_PB + 16 * 16 * 1024;
constexpr unsigned WS_DIAG = WS_Y + 2 * 4 * 32 * 112 * 4;
constexpr unsigned WS_END = WS_DIAG + 256 * 16 * 8;

struct Params {
    const unsigned short* x[2];
    const unsigned char* k[2];
    const unsigned char* v[2];
    const unsigned short* wq[2];
    const unsigned short* wo[2];
    unsigned short* out;
    unsigned char* ws;
};

__device__ __forceinline__ float asf(unsigned u) { return __builtin_bit_cast(float, u); }
__device__ __forceinline__ unsigned asu(float f) { return __builtin_bit_cast(unsigned, f); }
__device__ __forceinline__ float dot2(unsigned a, unsigned b, float c) {
    return __builtin_amdgcn_fdot2_f32_bf16(__builtin_bit_cast(bf16x2, a), __builtin_bit_cast(bf16x2, b), c, false);
}
__device__ __forceinline__ unsigned cvtpk(float lo, float hi) {
    bf16x2 r = {(__bf16)lo, (__bf16)hi};
    return __builtin_bit_cast(unsigned, r);
}
template <int CTRL> __device__ __forceinline__ float dpp(float x) {
    return asf((unsigned)__builtin_amdgcn_mov_dpp((int)asu(x), CTRL, 0xf, 0xf, true));
}
constexpr int XOR1 = 0xB1, XOR2 = 0x4E, HMIRROR = 0x141, ROR8 = 0x128;
__device__ __forceinline__ float bflo(unsigned u) { return asf(u << 16); }
__device__ __forceinline__ float bfhi(unsigned u) { return asf(u & 0xffff0000u); }
__device__ __forceinline__ float ex2(float x) { return __builtin_amdgcn_exp2f(x); }
constexpr int AUX_NT = 2, AUX_SC1 = 16;
__device__ __forceinline__ v4u ldnt(rsrc_t r, int voff, unsigned soff) { return __builtin_amdgcn_raw_buffer_load_b128(r, voff, soff, AUX_NT); }
__device__ __forceinline__ v4u ld128c(rsrc_t r, int voff) { return __builtin_amdgcn_raw_buffer_load_b128(r, voff, 0, AUX_SC1); }
__device__ __forceinline__ v2u ld64c(rsrc_t r, int voff) { return __builtin_amdgcn_raw_buffer_load_b64(r, voff, 0, AUX_SC1); }
__device__ __forceinline__ void st128c(v4u v, rsrc_t r, int voff) { __builtin_amdgcn_raw_buffer_store_b128(v, r, voff, 0, AUX_SC1); }
__device__ __forceinline__ void st64c(v2u v, rsrc_t r, int voff) { __builtin_amdgcn_raw_buffer_store_b64(v, r, voff, 0, AUX_SC1); }
__device__ __forceinline__ void st32c(unsigned v, rsrc_t r, int voff) { __builtin_amdgcn_raw_buffer_store_b32(v, r, voff, 0, AUX_SC1); }
__device__ __forceinline__ float swap32_add(float a, float b) {
    auto r = __builtin_amdgcn_permlane32_swap(asu(a), asu(b), false, false);
    return asf(r[0]) + asf(r[1]);
}
__device__ __forceinline__ float swap16_add(float a, float b) {
    auto r = __builtin_amdgcn_permlane16_swap(asu(a), asu(b), false, false);
    return asf(r[0]) + asf(r[1]);
}
__device__ __forceinline__ float x16_max(float x) { auto s = __builtin_amdgcn_permlane16_swap(asu(x), asu(x), false, false); return fmaxf(asf(s[0]), asf(s[1])); }
__device__ __forceinline__ float x32_max(float x) { auto s = __builtin_amdgcn_permlane32_swap(asu(x), asu(x), false, false); return fmaxf(asf(s[0]), asf(s[1])); }
__device__ __forceinline__ float x16_sum(float x) { auto s = __builtin_amdgcn_permlane16_swap(asu(x), asu(x), false, false); return asf(s[0]) + asf(s[1]); }
__device__ __forceinline__ float x32_sum(float x) { auto s = __builtin_amdgcn_permlane32_swap(asu(x), asu(x), false, false); return asf(s[0]) + asf(s[1]); }
__device__ __forceinline__ float xmax_groups(float x) { x = fmaxf(x, dpp<ROR8>(x)); return x32_max(x16_max(x)); }
__device__ __forceinline__ float xsum_groups(float x) { x = x + dpp<ROR8>(x); return x32_sum(x16_sum(x)); }
__device__ __forceinline__ float wave_max(float x) { x = fmaxf(x, dpp<XOR1>(x)); x = fmaxf(x, dpp<XOR2>(x)); x = fmaxf(x, dpp<HMIRROR>(x)); return xmax_groups(x); }
__device__ __forceinline__ float wave_sum(float x) { x += dpp<XOR1>(x); x += dpp<XOR2>(x); x += dpp<HMIRROR>(x); return xsum_groups(x); }
__device__ __forceinline__ bool all_lanes(bool ok) { return __builtin_amdgcn_ballot_w64(ok) == __builtin_amdgcn_ballot_w64(true); }
__device__ __forceinline__ bool valid4(v4u v) { return v.x != INVALID && v.y != INVALID && v.z != INVALID && v.w != INVALID; }
#define PIN(x) asm volatile("" : "+v"(x)::"memory")
#define PIN4(a, o) asm volatile("" : "+v"((a)[(o)]), "+v"((a)[(o) + 1]), "+v"((a)[(o) + 2]), "+v"((a)[(o) + 3])::"memory")
#define SB() __builtin_amdgcn_sched_barrier(0)
#define BARRIER() do { asm volatile("s_waitcnt lgkmcnt(0)" ::: "memory"); __builtin_amdgcn_s_barrier(); asm volatile("" ::: "memory"); } while (0)
#define STAMP(i) do { } while (0)
#define STAMP_FLUSH() do { } while (0)
#define STAMP_DECL

template <int G> struct State { float m[G], l[G]; f32x2 acc[G][16]; };

template <int G>
__device__ __forceinline__ void qk_piece(const v4u& kv, const v4u (&q)[G][4], int jj, float (&x)[G]) {
#pragma unroll
    for (int g = 0; g < G; ++g) {
        float a = x[g];
#pragma unroll
        for (int i = 0; i < 4; ++i) a = dot2(kv[i], q[g][jj][i], a);
        x[g] = a;
    }
}
template <int G>
__device__ __forceinline__ void softmax_step(float (&x0)[G], float (&x1)[G], State<G>& s, float (&p0)[G], float (&p1)[G]) {
#pragma unroll
    for (int g = 0; g < G; ++g) {
        float a = x0[g], b = x1[g];
        a += dpp<XOR1>(a); b += dpp<XOR1>(b);
        a += dpp<XOR2>(a); b += dpp<XOR2>(b);
        a += dpp<HMIRROR>(a); b += dpp<HMIRROR>(b);
        const unsigned pk = cvtpk(a, b);
        const float sa = bflo(pk) * SC, sb = bfhi(pk) * SC;
        const float mn = fmaxf(s.m[g], fmaxf(sa, sb));
        const float al = ex2(s.m[g] - mn);
        p0[g] = ex2(sa - mn); p1[g] = ex2(sb - mn);
        s.l[g] = fmaf(s.l[g], al, p0[g] + p1[g]);
        s.m[g] = mn;
#pragma unroll
        for (int i = 0; i < 16; ++i) s.acc[g][i] *= al;
    }
}
template <int G>
__device__ __forceinline__ void pv_piece(const v4u& vv, const float (&p)[G], int jj, State<G>& s) {
#pragma unroll
    for (int i = 0; i < 4; ++i) {
        const f32x2 vf = {bflo(vv[i]), bfhi(vv[i])};
#pragma unroll
        for (int g = 0; g < G; ++g) { const f32x2 pp = {p[g], p[g]}; s.acc[g][jj * 4 + i] = __builtin_elementwise_fma(vf, pp, s.acc[g][jj * 4 + i]); }
    }
}
constexpr int REC = 260;
template <int G>
__device__ __forceinline__ void flush_state(State<G>& s, LAS float* rec0, int lane) {
#pragma unroll
    for (int g = 0; g < G; ++g) {
        const float M = xmax_groups(s.m[g]);
        const float wt = ex2(s.m[g] - M);
        const float L = xsum_groups(s.l[g] * wt);
        float v[32];
#pragma unroll
        for (int i = 0; i < 16; ++i) { v[2 * i] = s.acc[g][i][0] * wt; v[2 * i + 1] = s.acc[g][i][1] * wt; }
        float a[16], b8[8], c4[4];
#pragma unroll
        for (int i = 0; i < 16; ++i) a[i] = swap32_add(v[i], v[i + 16]);
#pragma unroll
        for (int i = 0; i < 8; ++i) b8[i] = swap16_add(a[i], a[i + 8]);
        const bool hi8 = (lane & 8) != 0;
#pragma unroll
        for (int i = 0; i < 4; ++i) { const float keep = hi8 ? b8[i + 4] : b8[i], send = hi8 ? b8[i] : b8[i + 4]; c4[i] = keep + dpp<ROR8>(send); }
        LAS float* rec = rec0 + g * REC;
        const int d0 = 64 * (lane >> 4) + 8 * (lane & 7) + 4 * ((lane >> 3) & 1);
        *(LAS f32x4*)(rec + d0) = f32x4{c4[0], c4[1], c4[2], c4[3]};
        if (lane == 0) { rec[256] = M; rec[257] = L; }
    }
}
template <int G>
__device__ __forceinline__ void state_init(State<G>& s) {
#pragma unroll
    for (int g = 0; g < G; ++g) { s.m[g] = -1e30f; s.l[g] = 0.f;
#pragma unroll
        for (int i = 0; i < 16; ++i) s.acc[g][i] = f32x2{0.f, 0.f}; }
}

constexpr int L_REC = 0;
constexpr int L_RED = WAVES * 3 * REC;
constexpr int L_OV = L_RED + 8 * 128;
constexpr int L_Q = L_OV + 128;
constexpr int L_FLAG = L_Q + 384;
constexpr int L_WO = L_FLAG + 4;
constexpr int L_END = L_WO + 1792 * 4;

__global__ void __launch_bounds__(NTHR, LB) k_fused(Params p) {
    __shared__ __attribute__((aligned(16))) float lds[L_END];
    LAS float* L = (LAS float*)lds;
    const int b = blockIdx.x, t = threadIdx.x, lane = t & 63, w = __builtin_amdgcn_readfirstlane(t >> 6);
    const int gA = b >> 5, iA = b & 31, hB = b >> 4, jB = b & 15;
    const int it = (b & 7) * 32 + (b >> 3);
    const rsrc_t rws = __builtin_amdgcn_make_buffer_rsrc((void*)p.ws, 0, WS_END, 0x00020000);
    STAMP_DECL;
    STAMP(0);
    {
        int off = -1; unsigned val = INVALID;
        if (t < 64) off = WS_PA + ((2 * gA) * 32 + iA) * 1024 + t * 16;
        else if (t < 128) off = WS_PA + ((2 * gA + 1) * 32 + iA) * 1024 + (t - 64) * 16;
        else if (t < 192) off = WS_PB + (hB * 16 + jB) * 1024 + (t - 128) * 16;
        else if (t < 220) off = WS_Y + ((((it >> 7) * 4 + (it & 3)) * 32 + ((it >> 2) & 31)) * 112) * 4 + (t - 192) * 16;
        else if (t < 222) off = WS_ML + ((2 * gA + (t - 220)) * 32 + iA) * 16;
        else if (t == 222) off = WS_MLB + (hB * 16 + jB) * 16;
        else if (t == 223 && b < 16) { off = WS_CTR + b * 256; val = 0u; }
        if (off >= 0) st128c(v4u{val, val, val, val}, rws, off);
    }
    SB();
    const int qpr = it >> 7, qcol0 = (it & 127) * 32;
    const int rg = t >> 2, ch = t & 3;
    v4u wv[7]; unsigned xv[7];
    {
        const rsrc_t rwq = __builtin_amdgcn_make_buffer_rsrc((void*)p.wq[qpr], 0, DM * NQ * 2, 0x00020000);
        const rsrc_t rx = __builtin_amdgcn_make_buffer_rsrc((void*)p.x[qpr], 0, DM * 2, 0x00020000);
#pragma unroll
        for (int i = 0; i < 7; ++i) wv[i] = ldnt(rwq, rg * (NQ * 2) + qcol0 * 2 + ch * 16, i * (64 * NQ * 2));
#pragma unroll
        for (int i = 0; i < 7; ++i) xv[i] = __builtin_amdgcn_raw_buffer_load_b16(rx, rg * 2 + i * 128, 0, WQ_AUX);
    }
    v2u ep = {0u, 0u};
    if (w == 0) ep = ld64c(rws, WS_EPOCH);
    SB();
    const rsrc_t rkA = __builtin_amdgcn_make_buffer_rsrc((void*)p.k[0], 0, 8u * SK * DH * 2u, 0x00020000);
    const rsrc_t rvA = __builtin_amdgcn_make_buffer_rsrc((void*)p.v[0], 0, 8u * SK * DH * 2u, 0x00020000);
    const rsrc_t rkB = __builtin_amdgcn_make_buffer_rsrc((void*)p.k[1], 0, 16u * SK * DH * 2u, 0x00020000);
    const rsrc_t rvB = __builtin_amdgcn_make_buffer_rsrc((void*)p.v[1], 0, 16u * SK * DH * 2u, 0x00020000);
    const int vo = (lane >> 3) * 512 + (lane & 7) * 16;
    auto pc = [&](rsrc_t r, unsigned o, int j) -> v4u { return ldnt(r, vo + (j & 3) * 128, o + (j >> 2) * 4096); };
    const unsigned baseA = (unsigned)gA * (SK * 512u) + (unsigned)iA * (NTA * WAVES * TILE_B) + (unsigned)w * TILE_B;
    const unsigned headB = (unsigned)hB * (SK * 512u);
    const unsigned baseB = headB + (unsigned)jB * (NSB * WAVES * TILE_B) + (unsigned)w * TILE_B;
    v4u RK[8], RV[8];
    const int cpr = it >> 7, chq = (it >> 3) & 15, chf = (it >> 2) & 1, ccb = it & 3;
    asm volatile("s_waitcnt vmcnt(0)" : "+v"(wv[0]), "+v"(wv[1]), "+v"(wv[2]), "+v"(wv[3]), "+v"(wv[4]), "+v"(wv[5]), "+v"(wv[6]),
                 "+v"(xv[0]), "+v"(xv[1]), "+v"(xv[2]), "+v"(xv[3]), "+v"(xv[4]), "+v"(xv[5]), "+v"(xv[6]), "+v"(ep)::"memory");
    STAMP(10);
    if (w != 0) {
#pragma unroll
        for (int j = 0; j < 8; ++j) RK[j] = pc(rkA, baseA, j);
#pragma unroll
        for (int j = 0; j < 8; ++j) RV[j] = pc(rvA, baseA, j);
    }
    SB();
    {
        float acc[8];
#pragma unroll
        for (int e = 0; e < 8; ++e) acc[e] = 0.f;
#pragma unroll
        for (int i = 0; i < 7; ++i) {
            const float xf = asf(xv[i] << 16);
#pragma unroll
            for (int e = 0; e < 4; ++e) { acc[2 * e] = fmaf(xf, bflo(wv[i][e]), acc[2 * e]); acc[2 * e + 1] = fmaf(xf, bfhi(wv[i][e]), acc[2 * e + 1]); }
        }
#pragma unroll
        for (int e = 0; e < 8; ++e) { float v = acc[e]; v += dpp<0x124>(v);   v += dpp<ROR8>(v); v = x32_sum(x16_sum(v)); acc[e] = v; }
        if (lane < 4) {
            *(LAS f32x4*)(L + L_RED + w * 32 + lane * 8) = f32x4{acc[0], acc[1], acc[2], acc[3]};
            *(LAS f32x4*)(L + L_RED + w * 32 + lane * 8 + 4) = f32x4{acc[4], acc[5], acc[6], acc[7]};
        }
        if (t == 0) L[L_FLAG] = 0.f;
    }
    BARRIER();
    STAMP(11);
    u64 E = 0, tag = 0;
    if (w == 0) {
        E = ((u64)ep.y << 32) | ep.x;
        tag = (E + 1) & 0xffffffffffffull;
        if (lane < 32) {
            const float v = (L[L_RED + lane] + L[L_RED + 32 + lane]) + (L[L_RED + 64 + lane] + L[L_RED + 96 + lane]);
            const u64 g = (tag << 16) | (cvtpk(v, 0.f) & 0xffffu);
            st64c(v2u{(unsigned)g, (unsigned)(g >> 32)}, rws, WS_QG + (qpr * NQ + qcol0 + lane) * 8);
        } else if (lane == 32) {
            st64c(v2u{(unsigned)tag, (unsigned)(tag >> 32)}, rws, WS_READY + b * 8);
        }
        STAMP(1);
        const unsigned glo = (unsigned)tag << 16, ghi = (unsigned)(tag >> 16);
        v4u g[6];
        for (unsigned spins = 0;; ++spins) {
#pragma unroll
            for (int r = 0; r < 3; ++r) {
                const int row = r == 0 ? 2 * gA : r == 1 ? 2 * gA + 1 : 16 + hB;
                g[2 * r] = ld128c(rws, WS_QG + row * (DH * 8) + lane * 16);
                g[2 * r + 1] = ld128c(rws, WS_QG + row * (DH * 8) + 1024 + lane * 16);
            }
            bool ok = true;
#pragma unroll
            for (int r = 0; r < 6; ++r) ok = ok && (g[r].x & 0xffff0000u) == glo && g[r].y == ghi && (g[r].z & 0xffff0000u) == glo && g[r].w == ghi;
            if (all_lanes(ok) || spins > SPIN_CAP) break;
            __builtin_amdgcn_s_sleep(1);
        }
        LAS unsigned* qs = (LAS unsigned*)(L + L_Q);
#pragma unroll
        for (int r = 0; r < 6; ++r) qs[(r >> 1) * 128 + (r & 1) * 64 + lane] = (g[r].x & 0xffffu) | (g[r].z << 16);
        SB();
#pragma unroll
        for (int j = 0; j < 8; ++j) RK[j] = pc(rkA, baseA, j);
#pragma unroll
        for (int j = 0; j < 8; ++j) RV[j] = pc(rvA, baseA, j);
        SB();
    } else {
        const rsrc_t rwo = __builtin_amdgcn_make_buffer_rsrc((void*)p.wo[cpr], 0, DM * NQ * 2, 0x00020000);
        const int wob = ((chq * 256 + chf * 128) * DM + ccb * 112) * 2;
        v4u wo[10];
#pragma unroll
        for (int i = 0; i < 10; ++i) { const int e = t - 64 + 192 * i; const int ec = e < 1792 ? e : 0; wo[i] = ldnt(rwo, wob + (ec / 14) * (DM * 2) + (ec % 14) * 16, 0); }
#pragma unroll
        for (int i = 0; i < 10; ++i) { const int e = t - 64 + 192 * i; PIN(wo[i]); if (e < 1792) *(LAS v4u*)(L + L_WO + e * 4) = wo[i]; }
    }
    BARRIER();
    v4u qA[2][4];
    {
        const LAS unsigned char* qs = (const LAS unsigned char*)(L + L_Q);
#pragma unroll
        for (int jj = 0; jj < 4; ++jj) {
            const int d = 16 * ((lane & 7) + 8 * jj);
            qA[0][jj] = *(const LAS v4u*)(qs + d); qA[1][jj] = *(const LAS v4u*)(qs + 512 + d);
        }
    }
    GAS unsigned* ctr = (GAS unsigned*)(p.ws + WS_CTR) + hB * 64;
    asm volatile("" : "+v"(ctr));
    STAMP(2);
    {
        State<2> s; state_init(s);
        unsigned oc = baseA;
#pragma unroll 1
        for (int i = 0; i < NTA; ++i) {
            const bool last = (i == NTA - 1);
            const rsrc_t rkn = last ? rkB : rkA, rvn = last ? rvB : rvA;
            const unsigned on = last ? baseB : oc + WAVES * TILE_B;
            float x0[2] = {0.f, 0.f}, x1[2] = {0.f, 0.f};
#pragma unroll
            for (int j = 0; j < 8; ++j) {
                if (j < 4) { qk_piece<2>(RK[j], qA, j & 3, x0); PIN(x0[0]); PIN(x0[1]); } else { qk_piece<2>(RK[j], qA, j & 3, x1); PIN(x1[0]); PIN(x1[1]); }
                RK[j] = pc(rkn, on, j);
                SB();
            }
            float p0[2], p1[2];
            softmax_step<2>(x0, x1, s, p0, p1);
            SB();
#pragma unroll
            for (int j = 0; j < 8; ++j) {
                if (j < 4) pv_piece<2>(RV[j], p0, j & 3, s); else pv_piece<2>(RV[j], p1, j & 3, s);
                PIN4(s.acc[0], (j & 3) * 4); PIN4(s.acc[1], (j & 3) * 4);
                RV[j] = pc(rvn, on, j);
                SB();
            }
            oc = on;
        }
        flush_state<2>(s, L + L_REC + (w * 3) * REC, lane);
    }
    STAMP(3);
    if (w == 0) {
        const unsigned tlo = (unsigned)tag, thi = (unsigned)(tag >> 32);
        for (unsigned spins = 0;; ++spins) {
            const v4u rd0 = ld128c(rws, WS_READY + lane * 32), rd1 = ld128c(rws, WS_READY + lane * 32 + 16);
            const bool ok = rd0.x == tlo && rd0.y == thi && rd0.z == tlo && rd0.w == thi && rd1.x == tlo && rd1.y == thi && rd1.z == tlo && rd1.w == thi;
            if (all_lanes(ok) || spins > SPIN_CAP) break;
            __builtin_amdgcn_s_sleep(2);
        }
        *(volatile LAS unsigned*)(L + L_FLAG) = 1u;
        if (b == 0 && lane == 0) { const u64 e1 = E + 1; st64c(v2u{(unsigned)e1, (unsigned)(e1 >> 32)}, rws, WS_EPOCH); }
    }
    {
        v4u qB[1][4];
        {
            const LAS unsigned char* qs = (const LAS unsigned char*)(L + L_Q);
#pragma unroll
            for (int jj = 0; jj < 4; ++jj) qB[0][jj] = *(const LAS v4u*)(qs + 1024 + 16 * ((lane & 7) + 8 * jj));
        }
        State<1> s; state_init(s);
        const rsrc_t rnull = __builtin_amdgcn_make_buffer_rsrc((void*)p.k[1], 0, 0, 0x00020000);
        unsigned on = baseB + WAVES * TILE_B;
        int n = 1;
        bool have = true;
        bool more;
        do {
            unsigned tk = 0xffffffffu;
            if (NPOOL > 0 && n + 1 >= NSB && have) {
                if (n + 1 == NSB && w != 0) { for (unsigned spins = 0; *(volatile LAS unsigned*)(L + L_FLAG) == 0u && spins < (SPIN_CAP << 4); ++spins) __builtin_amdgcn_s_sleep(1); }
                if (lane == 0) tk = __hip_atomic_fetch_add(ctr, 1u, __ATOMIC_RELAXED, __HIP_MEMORY_SCOPE_AGENT);
            }
            const rsrc_t rk = have ? rkB : rnull, rv = have ? rvB : rnull;
            SB();
            float x0[1] = {0.f}, x1[1] = {0.f};
            float p0[1], p1[1];
#pragma unroll
            for (int j = 0; j < 8; ++j) {
                if (j < 4) { qk_piece<1>(RK[j], qB, j & 3, x0); PIN(x0[0]); } else { qk_piece<1>(RK[j], qB, j & 3, x1); PIN(x1[0]); }
                RK[j] = pc(rk, on, j);
                SB();
            }
            softmax_step<1>(x0, x1, s, p0, p1);
            SB();
#pragma unroll
            for (int j = 0; j < 8; ++j) {
                if (j < 4) pv_piece<1>(RV[j], p0, j & 3, s); else pv_piece<1>(RV[j], p1, j & 3, s);
                PIN4(s.acc[0], (j & 3) * 4);
                RV[j] = pc(rv, on, j);
                SB();
            }
            more = have;
            ++n;
            if (n < NSB) on += WAVES * TILE_B;
            else { const unsigned tt = __builtin_amdgcn_readfirstlane(tk); have = tt < (unsigned)NPOOL; on = headB + (POOL0 + tt) * TILE_B; }
        } while (more);
        flush_state<1>(s, L + L_REC + (w * 3 + 2) * REC, lane);
    }
    STAMP(8);
    BARRIER();
    STAMP(4);
    if (t < 192) {
        const int hs = w, d0 = lane * 4;
        float M = -1e30f;
#pragma unroll
        for (int ww = 0; ww < WAVES; ++ww) M = fmaxf(M, L[L_REC + (ww * 3 + hs) * REC + 256]);
        f32x4 a = {0.f, 0.f, 0.f, 0.f}; float Ls = 0.f;
#pragma unroll
        for (int ww = 0; ww < WAVES; ++ww) {
            const LAS float* rec = L + L_REC + (ww * 3 + hs) * REC;
            const float wt = ex2(rec[256] - M);
            a += *(const LAS f32x4*)(rec + d0) * wt; Ls = fmaf(rec[257], wt, Ls);
        }
        int dst, ml;
        if (hs < 2) { const int hq = 2 * gA + hs; dst = WS_PA + (hq * 32 + iA) * 1024; ml = WS_ML + (hq * 32 + iA) * 16; }
        else { dst = WS_PB + (hB * 16 + jB) * 1024; ml = WS_MLB + (hB * 16 + jB) * 16; }
        st128c(__builtin_bit_cast(v4u, a), rws, dst + lane * 16);
        if (lane == 0) st64c(v2u{asu(M), asu(Ls)}, rws, ml);
    }
    const int S = cpr == 0 ? 32 : 16;
    float wt, Lt;
    {
        const int mlb = (cpr == 0 ? WS_ML : WS_MLB) + chq * S * 16;
        const int sl = lane < S ? lane : 0;
        v2u ml;
        for (unsigned spins = 0;; ++spins) {
            ml = ld64c(rws, mlb + sl * 16);
            if (all_lanes(ml.x != INVALID && ml.y != INVALID) || spins > SPIN_CAP) break;
            __builtin_amdgcn_s_sleep(2);
        }
        const float ms = lane < S ? asf(ml.x) : -1e30f;
        const float M = wave_max(ms);
        wt = lane < S ? ex2(ms - M) : 0.f;
        Lt = wave_sum(wt * asf(ml.y));
    }
    STAMP(5);
    {
        const int c = t & 31, sg = t >> 5;
        const int pb = (cpr == 0 ? WS_PA : WS_PB) + chq * S * 1024 + chf * 512 + c * 16;
        v4u d[4];
        const int ni = S >> 3;
        for (unsigned spins = 0;; ++spins) {
            bool ok = true;
#pragma unroll
            for (int i = 0; i < 4; ++i) { const int s = sg + 8 * (i < ni ? i : 0); d[i] = ld128c(rws, pb + s * 1024); }
#pragma unroll
            for (int i = 0; i < 4; ++i) ok = ok && valid4(d[i]);
            if (all_lanes(ok) || spins > SPIN_CAP) break;
            __builtin_amdgcn_s_sleep(1);
        }
        f32x4 a = {0.f, 0.f, 0.f, 0.f};
#pragma unroll
        for (int i = 0; i < 4; ++i) {
            const float ws_ = i < ni ? __shfl(wt, sg + 8 * i) : 0.f;
            a += __builtin_bit_cast(f32x4, d[i]) * ws_;
        }
        *(LAS f32x4*)(L + L_RED + sg * 128 + c * 4) = a;
    }
    BARRIER();
    if (t < 128) {
        float v = 0.f;
#pragma unroll
        for (int g = 0; g < 8; ++g) v += L[L_RED + g * 128 + t];
        L[L_OV + t] = bflo(cvtpk(v / Lt, 0.f));
    }
    BARRIER();
    {
        float y[8];
#pragma unroll
        for (int e = 0; e < 8; ++e) y[e] = 0.f;
        const int org = t >> 4, occ = (t & 15) < 14 ? (t & 15) : 13;
#pragma unroll
        for (int i = 0; i < 8; ++i) {
            const float o = L[L_OV + org + 16 * i];
            const v4u wv_ = *(const LAS v4u*)(L + L_WO + ((org + 16 * i) * 14 + occ) * 4);
#pragma unroll
            for (int e = 0; e < 4; ++e) { y[2 * e] = fmaf(o, bflo(wv_[e]), y[2 * e]); y[2 * e + 1] = fmaf(o, bfhi(wv_[e]), y[2 * e + 1]); }
        }
#pragma unroll
        for (int e = 0; e < 8; ++e) y[e] = x32_sum(x16_sum(y[e]));
        if (lane < 14) {
            *(LAS f32x4*)(L + L_RED + w * 128 + lane * 8) = f32x4{y[0], y[1], y[2], y[3]};
            *(LAS f32x4*)(L + L_RED + w * 128 + lane * 8 + 4) = f32x4{y[4], y[5], y[6], y[7]};
        }
    }
    BARRIER();
    if (t < 28) {
        const f32x4 v = (*(const LAS f32x4*)(L + L_RED + t * 4) + *(const LAS f32x4*)(L + L_RED + 128 + t * 4)) +
                        (*(const LAS f32x4*)(L + L_RED + 256 + t * 4) + *(const LAS f32x4*)(L + L_RED + 384 + t * 4));
        st128c(__builtin_bit_cast(v4u, v), rws, WS_Y + (((cpr * 4 + ccb) * 32 + chq * 2 + chf) * 112) * 4 + t * 16);
    }
    STAMP(6);
    if ((it & 7) == 0 && t < 224) {
        const int r = it >> 3, fpr = r >> 4, fcb = (r >> 2) & 3, sub = r & 3;
        const int c = t >> 5, s = t & 31;
        const int off = WS_Y + (((fpr * 4 + fcb) * 32 + s) * 112 + sub * 28 + c * 4) * 4;
        v4u d;
        for (unsigned spins = 0;; ++spins) {
            d = ld128c(rws, off);
            if (all_lanes(valid4(d)) || spins > SPIN_CAP) break;
            __builtin_amdgcn_s_sleep(1);
        }
        float v[4] = {asf(d.x), asf(d.y), asf(d.z), asf(d.w)};
#pragma unroll
        for (int e = 0; e < 4; ++e) { float z = v[e]; z += dpp<XOR1>(z); z += dpp<XOR2>(z); z += dpp<HMIRROR>(z); z += dpp<ROR8>(z); v[e] = x16_sum(z); }
        if (s == 0) *(GAS v2u*)(p.out + fpr * DM + fcb * 112 + sub * 28 + c * 4) = v2u{cvtpk(v[0], v[1]), cvtpk(v[2], v[3])};
    }
    STAMP(7);
    STAMP_FLUSH();
}
}

extern "C" void kernel_launch(void* const* d_in, const int* in_sizes, int n_in, void* d_out, int out_size, void* d_ws, size_t ws_size,
                              hipStream_t stream) {
    Params p;
    p.x[0] = (const unsigned short*)d_in[0]; p.k[0] = (const unsigned char*)d_in[1]; p.v[0] = (const unsigned char*)d_in[2];
    p.wq[0] = (const unsigned short*)d_in[3]; p.wo[0] = (const unsigned short*)d_in[4];
    p.x[1] = (const unsigned short*)d_in[5]; p.k[1] = (const unsigned char*)d_in[6]; p.v[1] = (const unsigned char*)d_in[7];
    p.wq[1] = (const unsigned short*)d_in[8]; p.wo[1] = (const unsigned short*)d_in[9];
    p.out = (unsigned short*)d_out; p.ws = (unsigned char*)d_ws;
    k_fused<<<NWG, NTHR, 0, stream>>>(p);
}
```

```cpp
#define AUX_VB 0
#include <hip/hip_runtime.h>
#include <hip/hip_bf16.h>
#include <stdint.h>

#define GAS __attribute__((address_space(1)))
#define LAS __attribute__((address_space(3)))
typedef unsigned v4u __attribute__((ext_vector_type(4)));
typedef unsigned v2u __attribute__((ext_vector_type(2)));
typedef float f32x2 __attribute__((ext_vector_type(2)));
typedef float f32x4 __attribute__((ext_vector_type(4)));
typedef __bf16 bf16x2 __attribute__((ext_vector_type(2)));
typedef __amdgpu_buffer_rsrc_t rsrc_t;
typedef unsigned long long u64;

namespace {
constexpr int NWG = 256, WAVES = 4, NTHR = WAVES * 64;
constexpr int SK = 32768, DH = 256, DM = 448, NQ = 4096;
constexpr int TK = 16;
constexpr int TILE_B = TK * DH * 2;
constexpr int NTA = (SK / 32) / TK / WAVES;
constexpr int NTB_ALL = (SK / 16) / TK / WAVES;
#define LB 2
#define PREF 1
#define WQ_AUX 2
#define AUX_KA 2
#define AUX_VA 2
#define AUX_KB 2
#define NSB 20
constexpr int POOL0 = NSB * 16 * WAVES;
constexpr int NPOOL = (NTB_ALL - NSB) * 16 * WAVES;
constexpr float SC = 0.0625f * 1.4426950408889634f;
constexpr unsigned INVALID = 0xffffffffu;
constexpr unsigned SPIN_CAP = 1u << 18;

constexpr unsigned WS_EPOCH = 0;
constexpr unsigned WS_READY = 4096;
constexpr unsigned WS_CTR = 8192;
constexpr unsigned WS_QG = 16384;
constexpr unsigned WS_ML = WS_QG + 65536;
constexpr unsigned WS_MLB = WS_ML + 16 * 32 * 16;
constexpr unsigned WS_PA = 131072;
constexpr unsigned WS_PB = WS_PA + 16 * 32 * 1024;
constexpr unsigned WS_Y = WS_PB + 16 * 16 * 1024;
constexpr unsigned WS_DIAG = WS_Y + 2 * 4 * 32 * 112 * 4;
constexpr unsigned WS_END = WS_DIAG + 256 * 16 * 8;

struct Params {
    const unsigned short* x[2];
    const unsigned char* k[2];
    const unsigned char* v[2];
    const unsigned short* wq[2];
    const unsigned short* wo[2];
    unsigned short* out;
    unsigned char* ws;
};

__device__ __forceinline__ float asf(unsigned u) { return __builtin_bit_cast(float, u); }
__device__ __forceinline__ unsigned asu(float f) { return __builtin_bit_cast(unsigned, f); }
__device__ __forceinline__ float dot2(unsigned a, unsigned b, float c) {
    return __builtin_amdgcn_fdot2_f32_bf16(__builtin_bit_cast(bf16x2, a), __builtin_bit_cast(bf16x2, b), c, false);
}
__device__ __forceinline__ unsigned cvtpk(float lo, float hi) {
    bf16x2 r = {(__bf16)lo, (__bf16)hi};
    return __builtin_bit_cast(unsigned, r);
}
template <int CTRL> __device__ __forceinline__ float dpp(float x) {
    return asf((unsigned)__builtin_amdgcn_mov_dpp((int)asu(x), CTRL, 0xf, 0xf, true));
}
constexpr int XOR1 = 0xB1, XOR2 = 0x4E, HMIRROR = 0x141, ROR8 = 0x128;
__device__ __forceinline__ float bflo(unsigned u) { return asf(u << 16); }
__device__ __forceinline__ float bfhi(unsigned u) { return asf(u & 0xffff0000u); }
__device__ __forceinline__ float ex2(float x) { return __builtin_amdgcn_exp2f(x); }
constexpr int AUX_NT = 2, AUX_SC1 = 16;
__device__ __forceinline__ v4u ldnt(rsrc_t r, int voff, unsigned soff) { return __builtin_amdgcn_raw_buffer_load_b128(r, voff, soff, AUX_NT); }
__device__ __forceinline__ v4u ld128c(rsrc_t r, int voff) { return __builtin_amdgcn_raw_buffer_load_b128(r, voff, 0, AUX_SC1); }
__device__ __forceinline__ v2u ld64c(rsrc_t r, int voff) { return __builtin_amdgcn_raw_buffer_load_b64(r, voff, 0, AUX_SC1); }
__device__ __forceinline__ void st128c(v4u v, rsrc_t r, int voff) { __builtin_amdgcn_raw_buffer_store_b128(v, r, voff, 0, AUX_SC1); }
__device__ __forceinline__ void st64c(v2u v, rsrc_t r, int voff) { __builtin_amdgcn_raw_buffer_store_b64(v, r, voff, 0, AUX_SC1); }
__device__ __forceinline__ void st32c(unsigned v, rsrc_t r, int voff) { __builtin_amdgcn_raw_buffer_store_b32(v, r, voff, 0, AUX_SC1); }
__device__ __forceinline__ float swap32_add(float a, float b) {
    auto r = __builtin_amdgcn_permlane32_swap(asu(a), asu(b), false, false);
    return asf(r[0]) + asf(r[1]);
}
__device__ __forceinline__ float swap16_add(float a, float b) {
    auto r = __builtin_amdgcn_permlane16_swap(asu(a), asu(b), false, false);
    return asf(r[0]) + asf(r[1]);
}
__device__ __forceinline__ float x16_max(float x) { auto s = __builtin_amdgcn_permlane16_swap(asu(x), asu(x), false, false); return fmaxf(asf(s[0]), asf(s[1])); }
__device__ __forceinline__ float x32_max(float x) { auto s = __builtin_amdgcn_permlane32_swap(asu(x), asu(x), false, false); return fmaxf(asf(s[0]), asf(s[1])); }
__device__ __forceinline__ float x16_sum(float x) { auto s = __builtin_amdgcn_permlane16_swap(asu(x), asu(x), false, false); return asf(s[0]) + asf(s[1]); }
__device__ __forceinline__ float x32_sum(float x) { auto s = __builtin_amdgcn_permlane32_swap(asu(x), asu(x), false, false); return asf(s[0]) + asf(s[1]); }
__device__ __forceinline__ float xmax_groups(float x) { x = fmaxf(x, dpp<ROR8>(x)); return x32_max(x16_max(x)); }
__device__ __forceinline__ float xsum_groups(float x) { x = x + dpp<ROR8>(x); return x32_sum(x16_sum(x)); }
__device__ __forceinline__ float wave_max(float x) { x = fmaxf(x, dpp<XOR1>(x)); x = fmaxf(x, dpp<XOR2>(x)); x = fmaxf(x, dpp<HMIRROR>(x)); return xmax_groups(x); }
__device__ __forceinline__ float wave_sum(float x) { x += dpp<XOR1>(x); x += dpp<XOR2>(x); x += dpp<HMIRROR>(x); return xsum_groups(x); }
__device__ __forceinline__ bool all_lanes(bool ok) { return __builtin_amdgcn_ballot_w64(ok) == __builtin_amdgcn_ballot_w64(true); }
__device__ __forceinline__ bool valid4(v4u v) { return v.x != INVALID && v.y != INVALID && v.z != INVALID && v.w != INVALID; }
#define PIN(x) asm volatile("" : "+v"(x)::"memory")
#define PIN4(a, o) asm volatile("" : "+v"((a)[(o)]), "+v"((a)[(o) + 1]), "+v"((a)[(o) + 2]), "+v"((a)[(o) + 3])::"memory")
#define SB() __builtin_amdgcn_sched_barrier(0)
#define BARRIER() do { asm volatile("s_waitcnt lgkmcnt(0)" ::: "memory"); __builtin_amdgcn_s_barrier(); asm volatile("" ::: "memory"); } while (0)
#define STAMP(i) do { } while (0)
#define STAMP_FLUSH() do { } while (0)
#define STAMP_DECL

template <int G> struct State { float m[G], l[G]; f32x2 acc[G][16]; };

template <int G>
__device__ __forceinline__ void qk_piece(const v4u& kv, const v4u (&q)[G][4], int jj, float (&x)[G]) {
#pragma unroll
    for (int g = 0; g < G; ++g) {
        float a = x[g];
#pragma unroll
        for (int i = 0; i < 4; ++i) a = dot2(kv[i], q[g][jj][i], a);
        x[g] = a;
    }
}
template <int G>
__device__ __forceinline__ void softmax_step(float (&x0)[G], float (&x1)[G], State<G>& s, float (&p0)[G], float (&p1)[G]) {
#pragma unroll
    for (int g = 0; g < G; ++g) {
        float a = x0[g], b = x1[g];
        a += dpp<XOR1>(a); b += dpp<XOR1>(b);
        a += dpp<XOR2>(a); b += dpp<XOR2>(b);
        a += dpp<HMIRROR>(a); b += dpp<HMIRROR>(b);
        const unsigned pk = cvtpk(a, b);
        const float sa = bflo(pk) * SC, sb = bfhi(pk) * SC;
        const float mn = fmaxf(s.m[g], fmaxf(sa, sb));
        const float al = ex2(s.m[g] - mn);
        p0[g] = ex2(sa - mn); p1[g] = ex2(sb - mn);
        s.l[g] = fmaf(s.l[g], al, p0[g] + p1[g]);
        s.m[g] = mn;
#pragma unroll
        for (int i = 0; i < 16; ++i) s.acc[g][i] *= al;
    }
}
template <int G>
__device__ __forceinline__ void pv_piece(const v4u& vv, const float (&p)[G], int jj, State<G>& s) {
#pragma unroll
    for (int i = 0; i < 4; ++i) {
        const f32x2 vf = {bflo(vv[i]), bfhi(vv[i])};
#pragma unroll
        for (int g = 0; g < G; ++g) { const f32x2 pp = {p[g], p[g]}; s.acc[g][jj * 4 + i] = __builtin_elementwise_fma(vf, pp, s.acc[g][jj * 4 + i]); }
    }
}
constexpr int REC = 260;
template <int G>
__device__ __forceinline__ void flush_state(State<G>& s, LAS float* rec0, int lane) {
#pragma unroll
    for (int g = 0; g < G; ++g) {
        const float M = xmax_groups(s.m[g]);
        const float wt = ex2(s.m[g] - M);
        const float L = xsum_groups(s.l[g] * wt);
        float v[32];
#pragma unroll
        for (int i = 0; i < 16; ++i) { v[2 * i] = s.acc[g][i][0] * wt; v[2 * i + 1] = s.acc[g][i][1] * wt; }
        float a[16], b8[8], c4[4];
#pragma unroll
        for (int i = 0; i < 16; ++i) a[i] = swap32_add(v[i], v[i + 16]);
#pragma unroll
        for (int i = 0; i < 8; ++i) b8[i] = swap16_add(a[i], a[i + 8]);
        const bool hi8 = (lane & 8) != 0;
#pragma unroll
        for (int i = 0; i < 4; ++i) { const float keep = hi8 ? b8[i + 4] : b8[i], send = hi8 ? b8[i] : b8[i + 4]; c4[i] = keep + dpp<ROR8>(send); }
        LAS float* rec = rec0 + g * REC;
        const int d0 = 64 * (lane >> 4) + 8 * (lane & 7) + 4 * ((lane >> 3) & 1);
        *(LAS f32x4*)(rec + d0) = f32x4{c4[0], c4[1], c4[2], c4[3]};
        if (lane == 0) { rec[256] = M; rec[257] = L; }
    }
}
template <int G>
__device__ __forceinline__ void state_init(State<G>& s) {
#pragma unroll
    for (int g = 0; g < G; ++g) { s.m[g] = -1e30f; s.l[g] = 0.f;
#pragma unroll
        for (int i = 0; i < 16; ++i) s.acc[g][i] = f32x2{0.f, 0.f}; }
}

constexpr int L_REC = 0;
constexpr int L_RED = WAVES * 3 * REC;
constexpr int L_OV = L_RED + 8 * 128;
constexpr int L_Q = L_OV + 128;
constexpr int L_FLAG = L_Q + 384;
constexpr int L_WO = L_FLAG + 4;
constexpr int L_END = L_WO + 1792 * 4;

__global__ void __launch_bounds__(NTHR, LB) k_fused(Params p) {
    __shared__ __attribute__((aligned(16))) float lds[L_END];
    LAS float* L = (LAS float*)lds;
    const int b = blockIdx.x, t = threadIdx.x, lane = t & 63, w = __builtin_amdgcn_readfirstlane(t >> 6);
    const int gA = b >> 5, iA = b & 31, hB = b >> 4, jB = b & 15;
    const int it = (b & 7) * 32 + (b >> 3);
    const rsrc_t rws = __builtin_amdgcn_make_buffer_rsrc((void*)p.ws, 0, WS_END, 0x00020000);
    STAMP_DECL;
    STAMP(0);
    {
        int off = -1; unsigned val = INVALID;
        if (t < 64) off = WS_PA + ((2 * gA) * 32 + iA) * 1024 + t * 16;
        else if (t < 128) off = WS_PA + ((2 * gA + 1) * 32 + iA) * 1024 + (t - 64) * 16;
        else if (t < 192) off = WS_PB + (hB * 16 + jB) * 1024 + (t - 128) * 16;
        else if (t < 220) off = WS_Y + ((((it >> 7) * 4 + (it & 3)) * 32 + ((it >> 2) & 31)) * 112) * 4 + (t - 192) * 16;
        else if (t < 222) off = WS_ML + ((2 * gA + (t - 220)) * 32 + iA) * 16;
        else if (t == 222) off = WS_MLB + (hB * 16 + jB) * 16;
        else if (t == 223 && b < 16) { off = WS_CTR + b * 256; val = 0u; }
        if (off >= 0) st128c(v4u{val, val, val, val}, rws, off);
    }
    SB();
    const int qpr = it >> 7, qcol0 = (it & 127) * 32;
    const int rg = t >> 2, ch = t & 3;
    v4u wv[7]; unsigned xv[7];
    {
        const rsrc_t rwq = __builtin_amdgcn_make_buffer_rsrc((void*)p.wq[qpr], 0, DM * NQ * 2, 0x00020000);
        const rsrc_t rx = __builtin_amdgcn_make_buffer_rsrc((void*)p.x[qpr], 0, DM * 2, 0x00020000);
#pragma unroll
        for (int i = 0; i < 7; ++i) wv[i] = ldnt(rwq, rg * (NQ * 2) + qcol0 * 2 + ch * 16, i * (64 * NQ * 2));
#pragma unroll
        for (int i = 0; i < 7; ++i) xv[i] = __builtin_amdgcn_raw_buffer_load_b16(rx, rg * 2 + i * 128, 0, WQ_AUX);
    }
    v2u ep = {0u, 0u};
    if (w == 0) ep = ld64c(rws, WS_EPOCH);
    SB();
    const rsrc_t rkA = __builtin_amdgcn_make_buffer_rsrc((void*)p.k[0], 0, 8u * SK * DH * 2u, 0x00020000);
    const rsrc_t rvA = __builtin_amdgcn_make_buffer_rsrc((void*)p.v[0], 0, 8u * SK * DH * 2u, 0x00020000);
    const rsrc_t rkB = __builtin_amdgcn_make_buffer_rsrc((void*)p.k[1], 0, 16u * SK * DH * 2u, 0x00020000);
    const rsrc_t rvB = __builtin_amdgcn_make_buffer_rsrc((void*)p.v[1], 0, 16u * SK * DH * 2u, 0x00020000);
    const int vo = (lane >> 3) * 512 + (lane & 7) * 16;
    auto pc = [&](rsrc_t r, unsigned o, int j) -> v4u { return ldnt(r, vo + (j & 3) * 128, o + (j >> 2) * 4096); };
#define PCX(AUX, r, o, j) __builtin_amdgcn_raw_buffer_load_b128(r, vo + ((j) & 3) * 128, (o) + ((j) >> 2) * 4096, AUX)
    const unsigned baseA = (unsigned)gA * (SK * 512u) + (unsigned)iA * (NTA * WAVES * TILE_B) + (unsigned)w * TILE_B;
    const unsigned headB = (unsigned)hB * (SK * 512u);
    const unsigned baseB = headB + (unsigned)jB * (NSB * WAVES * TILE_B) + (unsigned)w * TILE_B;
    v4u RK[8], RV[8];
    const int cpr = it >> 7, chq = (it >> 3) & 15, chf = (it >> 2) & 1, ccb = it & 3;
    asm volatile("s_waitcnt vmcnt(0)" : "+v"(wv[0]), "+v"(wv[1]), "+v"(wv[2]), "+v"(wv[3]), "+v"(wv[4]), "+v"(wv[5]), "+v"(wv[6]),
                 "+v"(xv[0]), "+v"(xv[1]), "+v"(xv[2]), "+v"(xv[3]), "+v"(xv[4]), "+v"(xv[5]), "+v"(xv[6]), "+v"(ep)::"memory");
    STAMP(10);
    if (w != 0) {
#pragma unroll
        for (int j = 0; j < 8; ++j) RK[j] = pc(rkA, baseA, j);
#pragma unroll
        for (int j = 0; j < 8; ++j) RV[j] = pc(rvA, baseA, j);
    }
    SB();
    {
        float acc[8];
#pragma unroll
        for (int e = 0; e < 8; ++e) acc[e] = 0.f;
#pragma unroll
        for (int i = 0; i < 7; ++i) {
            const float xf = asf(xv[i] << 16);
#pragma unroll
            for (int e = 0; e < 4; ++e) { acc[2 * e] = fmaf(xf, bflo(wv[i][e]), acc[2 * e]); acc[2 * e + 1] = fmaf(xf, bfhi(wv[i][e]), acc[2 * e + 1]); }
        }
#pragma unroll
        for (int e = 0; e < 8; ++e) { float v = acc[e]; v += dpp<0x124>(v);   v += dpp<ROR8>(v); v = x32_sum(x16_sum(v)); acc[e] = v; }
        if (lane < 4) {
            *(LAS f32x4*)(L + L_RED + w * 32 + lane * 8) = f32x4{acc[0], acc[1], acc[2], acc[3]};
            *(LAS f32x4*)(L + L_RED + w * 32 + lane * 8 + 4) = f32x4{acc[4], acc[5], acc[6], acc[7]};
        }
        if (t == 0) L[L_FLAG] = 0.f;
    }
    BARRIER();
    STAMP(11);
    u64 E = 0, tag = 0;
    if (w == 0) {
        E = ((u64)ep.y << 32) | ep.x;
        tag = (E + 1) & 0xffffffffffffull;
        if (lane < 32) {
            const float v = (L[L_RED + lane] + L[L_RED + 32 + lane]) + (L[L_RED + 64 + lane] + L[L_RED + 96 + lane]);
            const u64 g = (tag << 16) | (cvtpk(v, 0.f) & 0xffffu);
            st64c(v2u{(unsigned)g, (unsigned)(g >> 32)}, rws, WS_QG + (qpr * NQ + qcol0 + lane) * 8);
        } else if (lane == 32) {
            st64c(v2u{(unsigned)tag, (unsigned)(tag >> 32)}, rws, WS_READY + b * 8);
        }
        STAMP(1);
        const unsigned glo = (unsigned)tag << 16, ghi = (unsigned)(tag >> 16);
        v4u g[6];
        for (unsigned spins = 0;; ++spins) {
#pragma unroll
            for (int r = 0; r < 3; ++r) {
                const int row = r == 0 ? 2 * gA : r == 1 ? 2 * gA + 1 : 16 + hB;
                g[2 * r] = ld128c(rws, WS_QG + row * (DH * 8) + lane * 16);
                g[2 * r + 1] = ld128c(rws, WS_QG + row * (DH * 8) + 1024 + lane * 16);
            }
            bool ok = true;
#pragma unroll
            for (int r = 0; r < 6; ++r) ok = ok && (g[r].x & 0xffff0000u) == glo && g[r].y == ghi && (g[r].z & 0xffff0000u) == glo && g[r].w == ghi;
            if (all_lanes(ok) || spins > SPIN_CAP) break;
            __builtin_amdgcn_s_sleep(1);
        }
        LAS unsigned* qs = (LAS unsigned*)(L + L_Q);
#pragma unroll
        for (int r = 0; r < 6; ++r) qs[(r >> 1) * 128 + (r & 1) * 64 + lane] = (g[r].x & 0xffffu) | (g[r].z << 16);
        SB();
#pragma unroll
        for (int j = 0; j < 8; ++j) RK[j] = pc(rkA, baseA, j);
#pragma unroll
        for (int j = 0; j < 8; ++j) RV[j] = pc(rvA, baseA, j);
        SB();
    } else {
        const rsrc_t rwo = __builtin_amdgcn_make_buffer_rsrc((void*)p.wo[cpr], 0, DM * NQ * 2, 0x00020000);
        const int wob = ((chq * 256 + chf * 128) * DM + ccb * 112) * 2;
        v4u wo[10];
#pragma unroll
        for (int i = 0; i < 10; ++i) { const int e = t - 64 + 192 * i; const int ec = e < 1792 ? e : 0; wo[i] = ldnt(rwo, wob + (ec / 14) * (DM * 2) + (ec % 14) * 16, 0); }
#pragma unroll
        for (int i = 0; i < 10; ++i) { const int e = t - 64 + 192 * i; PIN(wo[i]); if (e < 1792) *(LAS v4u*)(L + L_WO + e * 4) = wo[i]; }
    }
    BARRIER();
    v4u qA[2][4];
    {
        const LAS unsigned char* qs = (const LAS unsigned char*)(L + L_Q);
#pragma unroll
        for (int jj = 0; jj < 4; ++jj) {
            const int d = 16 * ((lane & 7) + 8 * jj);
            qA[0][jj] = *(const LAS v4u*)(qs + d); qA[1][jj] = *(const LAS v4u*)(qs + 512 + d);
        }
    }
    GAS unsigned* ctr = (GAS unsigned*)(p.ws + WS_CTR) + hB * 64;
    asm volatile("" : "+v"(ctr));
    STAMP(2);
    {
        State<2> s; state_init(s);
        unsigned oc = baseA;
#pragma unroll 1
        for (int i = 0; i < NTA; ++i) {
            const bool last = (i == NTA - 1);
            const rsrc_t rkn = last ? rkB : rkA, rvn = last ? rvB : rvA;
            const unsigned on = last ? baseB : oc + WAVES * TILE_B;
            float x0[2] = {0.f, 0.f}, x1[2] = {0.f, 0.f};
#pragma unroll
            for (int j = 0; j < 8; ++j) {
                if (j < 4) { qk_piece<2>(RK[j], qA, j & 3, x0); PIN(x0[0]); PIN(x0[1]); } else { qk_piece<2>(RK[j], qA, j & 3, x1); PIN(x1[0]); PIN(x1[1]); }
                RK[j] = PCX(AUX_KA, rkn, on, j);
                SB();
            }
            float p0[2], p1[2];
            softmax_step<2>(x0, x1, s, p0, p1);
            SB();
#pragma unroll
            for (int j = 0; j < 8; ++j) {
                if (j < 4) pv_piece<2>(RV[j], p0, j & 3, s); else pv_piece<2>(RV[j], p1, j & 3, s);
                PIN4(s.acc[0], (j & 3) * 4); PIN4(s.acc[1], (j & 3) * 4);
                RV[j] = PCX(AUX_VA, rvn, on, j);
                SB();
            }
            oc = on;
        }
        flush_state<2>(s, L + L_REC + (w * 3) * REC, lane);
    }
    STAMP(3);
    if (w == 0) {
        const unsigned tlo = (unsigned)tag, thi = (unsigned)(tag >> 32);
        for (unsigned spins = 0;; ++spins) {
            const v4u rd0 = ld128c(rws, WS_READY + lane * 32), rd1 = ld128c(rws, WS_READY + lane * 32 + 16);
            const bool ok = rd0.x == tlo && rd0.y == thi && rd0.z == tlo && rd0.w == thi && rd1.x == tlo && rd1.y == thi && rd1.z == tlo && rd1.w == thi;
            if (all_lanes(ok) || spins > SPIN_CAP) break;
            __builtin_amdgcn_s_sleep(2);
        }
        *(volatile LAS unsigned*)(L + L_FLAG) = 1u;
        if (b == 0 && lane == 0) { const u64 e1 = E + 1; st64c(v2u{(unsigned)e1, (unsigned)(e1 >> 32)}, rws, WS_EPOCH); }
    }
    {
        v4u qB[1][4];
        {
            const LAS unsigned char* qs = (const LAS unsigned char*)(L + L_Q);
#pragma unroll
            for (int jj = 0; jj < 4; ++jj) qB[0][jj] = *(const LAS v4u*)(qs + 1024 + 16 * ((lane & 7) + 8 * jj));
        }
        State<1> s; state_init(s);
        const rsrc_t rnull = __builtin_amdgcn_make_buffer_rsrc((void*)p.k[1], 0, 0, 0x00020000);
        unsigned on = baseB + WAVES * TILE_B;
        int n = 1;
        bool have = true;
        bool more;
        do {
            unsigned tk = 0xffffffffu;
            if (NPOOL > 0 && n + 1 >= NSB && have) {
                if (n + 1 == NSB && w != 0) { for (unsigned spins = 0; *(volatile LAS unsigned*)(L + L_FLAG) == 0u && spins < (SPIN_CAP << 4); ++spins) __builtin_amdgcn_s_sleep(1); }
                if (lane == 0) tk = __hip_atomic_fetch_add(ctr, 1u, __ATOMIC_RELAXED, __HIP_MEMORY_SCOPE_AGENT);
            }
            const rsrc_t rk = have ? rkB : rnull, rv = have ? rvB : rnull;
            SB();
            float x0[1] = {0.f}, x1[1] = {0.f};
            float p0[1], p1[1];
#pragma unroll
            for (int j = 0; j < 8; ++j) {
                if (j < 4) { qk_piece<1>(RK[j], qB, j & 3, x0); PIN(x0[0]); } else { qk_piece<1>(RK[j], qB, j & 3, x1); PIN(x1[0]); }
                RK[j] = PCX(AUX_KB, rk, on, j);
                SB();
            }
            softmax_step<1>(x0, x1, s, p0, p1);
            SB();
#pragma unroll
            for (int j = 0; j < 8; ++j) {
                if (j < 4) pv_piece<1>(RV[j], p0, j & 3, s); else pv_piece<1>(RV[j], p1, j & 3, s);
                PIN4(s.acc[0], (j & 3) * 4);
                RV[j] = PCX(AUX_VB, rv, on, j);
                SB();
            }
            more = have;
            ++n;
            if (n < NSB) on += WAVES * TILE_B;
            else { const unsigned tt = __builtin_amdgcn_readfirstlane(tk); have = tt < (unsigned)NPOOL; on = headB + (POOL0 + tt) * TILE_B; }
        } while (more);
        flush_state<1>(s, L + L_REC + (w * 3 + 2) * REC, lane);
    }
    STAMP(8);
    BARRIER();
    STAMP(4);
    if (t < 192) {
        const int hs = w, d0 = lane * 4;
        float M = -1e30f;
#pragma unroll
        for (int ww = 0; ww < WAVES; ++ww) M = fmaxf(M, L[L_REC + (ww * 3 + hs) * REC + 256]);
        f32x4 a = {0.f, 0.f, 0.f, 0.f}; float Ls = 0.f;
#pragma unroll
        for (int ww = 0; ww < WAVES; ++ww) {
            const LAS float* rec = L + L_REC + (ww * 3 + hs) * REC;
            const float wt = ex2(rec[256] - M);
            a += *(const LAS f32x4*)(rec + d0) * wt; Ls = fmaf(rec[257], wt, Ls);
        }
        int dst, ml;
        if (hs < 2) { const int hq = 2 * gA + hs; dst = WS_PA + (hq * 32 + iA) * 1024; ml = WS_ML + (hq * 32 + iA) * 16; }
        else { dst = WS_PB + (hB * 16 + jB) * 1024; ml = WS_MLB + (hB * 16 + jB) * 16; }
        st128c(__builtin_bit_cast(v4u, a), rws, dst + lane * 16);
        if (lane == 0) st64c(v2u{asu(M), asu(Ls)}, rws, ml);
    }
    const int S = cpr == 0 ? 32 : 16;
    float wt, Lt;
    {
        const int mlb = (cpr == 0 ? WS_ML : WS_MLB) + chq * S * 16;
        const int sl = lane < S ? lane : 0;
        v2u ml;
        for (unsigned spins = 0;; ++spins) {
            ml = ld64c(rws, mlb + sl * 16);
            if (all_lanes(ml.x != INVALID && ml.y != INVALID) || spins > SPIN_CAP) break;
            __builtin_amdgcn_s_sleep(2);
        }
        const float ms = lane < S ? asf(ml.x) : -1e30f;
        const float M = wave_max(ms);
        wt = lane < S ? ex2(ms - M) : 0.f;
        Lt = wave_sum(wt * asf(ml.y));
    }
    STAMP(5);
    {
        const int c = t & 31, sg = t >> 5;
        const int pb = (cpr == 0 ? WS_PA : WS_PB) + chq * S * 1024 + chf * 512 + c * 16;
        v4u d[4];
        const int ni = S >> 3;
        for (unsigned spins = 0;; ++spins) {
            bool ok = true;
#pragma unroll
            for (int i = 0; i < 4; ++i) { const int s = sg + 8 * (i < ni ? i : 0); d[i] = ld128c(rws, pb + s * 1024); }
#pragma unroll
            for (int i = 0; i < 4; ++i) ok = ok && valid4(d[i]);
            if (all_lanes(ok) || spins > SPIN_CAP) break;
            __builtin_amdgcn_s_sleep(1);
        }
        f32x4 a = {0.f, 0.f, 0.f, 0.f};
#pragma unroll
        for (int i = 0; i < 4; ++i) {
            const float ws_ = i < ni ? __shfl(wt, sg + 8 * i) : 0.f;
            a += __builtin_bit_cast(f32x4, d[i]) * ws_;
        }
        *(LAS f32x4*)(L + L_RED + sg * 128 + c * 4) = a;
    }
    BARRIER();
    if (t < 128) {
        float v = 0.f;
#pragma unroll
        for (int g = 0; g < 8; ++g) v += L[L_RED + g * 128 + t];
        L[L_OV + t] = bflo(cvtpk(v / Lt, 0.f));
    }
    BARRIER();
    {
        float y[8];
#pragma unroll
        for (int e = 0; e < 8; ++e) y[e] = 0.f;
        const int org = t >> 4, occ = (t & 15) < 14 ? (t & 15) : 13;
#pragma unroll
        for (int i = 0; i < 8; ++i) {
            const float o = L[L_OV + org + 16 * i];
            const v4u wv_ = *(const LAS v4u*)(L + L_WO + ((org + 16 * i) * 14 + occ) * 4);
#pragma unroll
            for (int e = 0; e < 4; ++e) { y[2 * e] = fmaf(o, bflo(wv_[e]), y[2 * e]); y[2 * e + 1] = fmaf(o, bfhi(wv_[e]), y[2 * e + 1]); }
        }
#pragma unroll
        for (int e = 0; e < 8; ++e) y[e] = x32_sum(x16_sum(y[e]));
        if (lane < 14) {
            *(LAS f32x4*)(L + L_RED + w * 128 + lane * 8) = f32x4{y[0], y[1], y[2], y[3]};
            *(LAS f32x4*)(L + L_RED + w * 128 + lane * 8 + 4) = f32x4{y[4], y[5], y[6], y[7]};
        }
    }
    BARRIER();
    if (t < 28) {
        const f32x4 v = (*(const LAS f32x4*)(L + L_RED + t * 4) + *(const LAS f32x4*)(L + L_RED + 128 + t * 4)) +
                        (*(const LAS f32x4*)(L + L_RED + 256 + t * 4) + *(const LAS f32x4*)(L + L_RED + 384 + t * 4));
        st128c(__builtin_bit_cast(v4u, v), rws, WS_Y + (((cpr * 4 + ccb) * 32 + chq * 2 + chf) * 112) * 4 + t * 16);
    }
    STAMP(6);
    if ((it & 7) == 0 && t < 224) {
        const int r = it >> 3, fpr = r >> 4, fcb = (r >> 2) & 3, sub = r & 3;
        const int c = t >> 5, s = t & 31;
        const int off = WS_Y + (((fpr * 4 + fcb) * 32 + s) * 112 + sub * 28 + c * 4) * 4;
        v4u d;
        for (unsigned spins = 0;; ++spins) {
            d = ld128c(rws, off);
            if (all_lanes(valid4(d)) || spins > SPIN_CAP) break;
            __builtin_amdgcn_s_sleep(1);
        }
        float v[4] = {asf(d.x), asf(d.y), asf(d.z), asf(d.w)};
#pragma unroll
        for (int e = 0; e < 4; ++e) { float z = v[e]; z += dpp<XOR1>(z); z += dpp<XOR2>(z); z += dpp<HMIRROR>(z); z += dpp<ROR8>(z); v[e] = x16_sum(z); }
        if (s == 0) *(GAS v2u*)(p.out + fpr * DM + fcb * 112 + sub * 28 + c * 4) = v2u{cvtpk(v[0], v[1]), cvtpk(v[2], v[3])};
    }
    STAMP(7);
    STAMP_FLUSH();
}
}

extern "C" void kernel_launch(void* const* d_in, const int* in_sizes, int n_in, void* d_out, int out_size, void* d_ws, size_t ws_size,
                              hipStream_t stream) {
    Params p;
    p.x[0] = (const unsigned short*)d_in[0]; p.k[0] = (const unsigned char*)d_in[1]; p.v[0] = (const unsigned char*)d_in[2];
    p.wq[0] = (const unsigned short*)d_in[3]; p.wo[0] = (const unsigned short*)d_in[4];
    p.x[1] = (const unsigned short*)d_in[5]; p.k[1] = (const unsigned char*)d_in[6]; p.v[1] = (const unsigned char*)d_in[7];
    p.wq[1] = (const unsigned short*)d_in[8]; p.wo[1] = (const unsigned short*)d_in[9];
    p.out = (unsigned short*)d_out; p.ws = (unsigned char*)d_ws;
    k_fused<<<NWG, NTHR, 0, stream>>>(p);
}
```

```cpp
#include <hip/hip_runtime.h>
#include <hip/hip_bf16.h>
#include <stdint.h>

#define GAS __attribute__((address_space(1)))
#define LAS __attribute__((address_space(3)))
typedef unsigned v4u __attribute__((ext_vector_type(4)));
typedef unsigned v2u __attribute__((ext_vector_type(2)));
typedef float f32x2 __attribute__((ext_vector_type(2)));
typedef float f32x4 __attribute__((ext_vector_type(4)));
typedef __bf16 bf16x2 __attribute__((ext_vector_type(2)));
typedef __amdgpu_buffer_rsrc_t rsrc_t;
typedef unsigned long long u64;

namespace {
constexpr int NWG = 256, WAVES = 4, NTHR = WAVES * 64;
constexpr int SK = 32768, DH = 256, DM = 448, NQ = 4096;
constexpr int TK = 16;
constexpr int TILE_B = TK * DH * 2;
constexpr int NTA = (SK / 32) / TK / WAVES;
constexpr int NTB_ALL = (SK / 16) / TK / WAVES;
#define LB 2
#define PREF 3
#define WQ_AUX 2
#define AUX_KA 2
#define AUX_VA 2
#define AUX_KB 2
#define AUX_VB 2
#define POLL_SLEEP 2
#define PATA 0
#define PATB 0
#define EPOCH_AUX 16
#define NSB 20
constexpr int POOL0 = NSB * 16 * WAVES;
constexpr int NPOOL = (NTB_ALL - NSB) * 16 * WAVES;
constexpr float SC = 0.0625f * 1.4426950408889634f;
constexpr unsigned INVALID = 0xffffffffu;
constexpr unsigned SPIN_CAP = 1u << 18;

constexpr unsigned WS_EPOCH = 0;
constexpr unsigned WS_READY = 4096;
constexpr unsigned WS_CTR = 8192;
constexpr unsigned WS_QG = 16384;
constexpr unsigned WS_ML = WS_QG + 65536;
constexpr unsigned WS_MLB = WS_ML + 16 * 32 * 16;
constexpr unsigned WS_PA = 131072;
constexpr unsigned WS_PB = WS_PA + 16 * 32 * 1024;
constexpr unsigned WS_Y = WS_PB + 16 * 16 * 1024;
constexpr unsigned WS_DIAG = WS_Y + 2 * 4 * 32 * 112 * 4;
constexpr unsigned WS_END = WS_DIAG + 256 * 16 * 8;

struct Params {
    const unsigned short* x[2];
    const unsigned char* k[2];
    const unsigned char* v[2];
    const unsigned short* wq[2];
    const unsigned short* wo[2];
    unsigned short* out;
    unsigned char* ws;
};

__device__ __forceinline__ float asf(unsigned u) { return __builtin_bit_cast(float, u); }
__device__ __forceinline__ unsigned asu(float f) { return __builtin_bit_cast(unsigned, f); }
__device__ __forceinline__ float dot2(unsigned a, unsigned b, float c) {
    return __builtin_amdgcn_fdot2_f32_bf16(__builtin_bit_cast(bf16x2, a), __builtin_bit_cast(bf16x2, b), c, false);
}
__device__ __forceinline__ unsigned cvtpk(float lo, float hi) {
    bf16x2 r = {(__bf16)lo, (__bf16)hi};
    return __builtin_bit_cast(unsigned, r);
}
template <int CTRL> __device__ __forceinline__ float dpp(float x) {
    return asf((unsigned)__builtin_amdgcn_mov_dpp((int)asu(x), CTRL, 0xf, 0xf, true));
}
constexpr int XOR1 = 0xB1, XOR2 = 0x4E, HMIRROR = 0x141, ROR8 = 0x128;
__device__ __forceinline__ float bflo(unsigned u) { return asf(u << 16); }
__device__ __forceinline__ float bfhi(unsigned u) { return asf(u & 0xffff0000u); }
__device__ __forceinline__ float ex2(float x) { return __builtin_amdgcn_exp2f(x); }
constexpr int AUX_NT = 2, AUX_SC1 = 16;
__device__ __forceinline__ v4u ldnt(rsrc_t r, int voff, unsigned soff) { return __builtin_amdgcn_raw_buffer_load_b128(r, voff, soff, AUX_NT); }
__device__ __forceinline__ v4u ld128c(rsrc_t r, int voff) { return __builtin_amdgcn_raw_buffer_load_b128(r, voff, 0, AUX_SC1); }
__device__ __forceinline__ v2u ld64c(rsrc_t r, int voff) { return __builtin_amdgcn_raw_buffer_load_b64(r, voff, 0, AUX_SC1); }
__device__ __forceinline__ void st128c(v4u v, rsrc_t r, int voff) { __builtin_amdgcn_raw_buffer_store_b128(v, r, voff, 0, AUX_SC1); }
__device__ __forceinline__ void st64c(v2u v, rsrc_t r, int voff) { __builtin_amdgcn_raw_buffer_store_b64(v, r, voff, 0, AUX_SC1); }
__device__ __forceinline__ void st32c(unsigned v, rsrc_t r, int voff) { __builtin_amdgcn_raw_buffer_store_b32(v, r, voff, 0, AUX_SC1); }
__device__ __forceinline__ float swap32_add(float a, float b) {
    auto r = __builtin_amdgcn_permlane32_swap(asu(a), asu(b), false, false);
    return asf(r[0]) + asf(r[1]);
}
__device__ __forceinline__ float swap16_add(float a, float b) {
    auto r = __builtin_amdgcn_permlane16_swap(asu(a), asu(b), false, false);
    return asf(r[0]) + asf(r[1]);
}
__device__ __forceinline__ float x16_max(float x) { auto s = __builtin_amdgcn_permlane16_swap(asu(x), asu(x), false, false); return fmaxf(asf(s[0]), asf(s[1])); }
__device__ __forceinline__ float x32_max(float x) { auto s = __builtin_amdgcn_permlane32_swap(asu(x), asu(x), false, false); return fmaxf(asf(s[0]), asf(s[1])); }
__device__ __forceinline__ float x16_sum(float x) { auto s = __builtin_amdgcn_permlane16_swap(asu(x), asu(x), false, false); return asf(s[0]) + asf(s[1]); }
__device__ __forceinline__ float x32_sum(float x) { auto s = __builtin_amdgcn_permlane32_swap(asu(x), asu(x), false, false); return asf(s[0]) + asf(s[1]); }
__device__ __forceinline__ float xmax_groups(float x) { x = fmaxf(x, dpp<ROR8>(x)); return x32_max(x16_max(x)); }
__device__ __forceinline__ float xsum_groups(float x) { x = x + dpp<ROR8>(x); return x32_sum(x16_sum(x)); }
__device__ __forceinline__ float wave_max(float x) { x = fmaxf(x, dpp<XOR1>(x)); x = fmaxf(x, dpp<XOR2>(x)); x = fmaxf(x, dpp<HMIRROR>(x)); return xmax_groups(x); }
__device__ __forceinline__ float wave_sum(float x) { x += dpp<XOR1>(x); x += dpp<XOR2>(x); x += dpp<HMIRROR>(x); return xsum_groups(x); }
__device__ __forceinline__ bool all_lanes(bool ok) { return __builtin_amdgcn_ballot_w64(ok) == __builtin_amdgcn_ballot_w64(true); }
__device__ __forceinline__ bool valid4(v4u v) { return v.x != INVALID && v.y != INVALID && v.z != INVALID && v.w != INVALID; }
#define PIN(x) asm volatile("" : "+v"(x)::"memory")
#define PIN4(a, o) asm volatile("" : "+v"((a)[(o)]), "+v"((a)[(o) + 1]), "+v"((a)[(o) + 2]), "+v"((a)[(o) + 3])::"memory")
#define SB() __builtin_amdgcn_sched_barrier(0)
#define BARRIER() do { asm volatile("s_waitcnt lgkmcnt(0)" ::: "memory"); __builtin_amdgcn_s_barrier(); asm volatile("" ::: "memory"); } while (0)
#define STAMP(i) do { } while (0)
#define STAMP_FLUSH() do { } while (0)
#define STAMP_DECL

template <int G> struct State { float m[G], l[G]; f32x2 acc[G][16]; };

template <int G>
__device__ __forceinline__ void qk_piece(const v4u& kv, const v4u (&q)[G][4], int jj, float (&x)[G]) {
#pragma unroll
    for (int g = 0; g < G; ++g) {
        float a = x[g];
#pragma unroll
        for (int i = 0; i < 4; ++i) a = dot2(kv[i], q[g][jj][i], a);
        x[g] = a;
    }
}
template <int G>
__device__ __forceinline__ void softmax_step(float (&x0)[G], float (&x1)[G], State<G>& s, float (&p0)[G], float (&p1)[G]) {
#pragma unroll
    for (int g = 0; g < G; ++g) {
        float a = x0[g], b = x1[g];
        a += dpp<XOR1>(a); b += dpp<XOR1>(b);
        a += dpp<XOR2>(a); b += dpp<XOR2>(b);
        a += dpp<HMIRROR>(a); b += dpp<HMIRROR>(b);
        const unsigned pk = cvtpk(a, b);
        const float sa = bflo(pk) * SC, sb = bfhi(pk) * SC;
        const float mn = fmaxf(s.m[g], fmaxf(sa, sb));
        const float al = ex2(s.m[g] - mn);
        p0[g] = ex2(sa - mn); p1[g] = ex2(sb - mn);
        s.l[g] = fmaf(s.l[g], al, p0[g] + p1[g]);
        s.m[g] = mn;
#pragma unroll
        for (int i = 0; i < 16; ++i) s.acc[g][i] *= al;
    }
}
template <int G>
__device__ __forceinline__ void pv_piece(const v4u& vv, const float (&p)[G], int jj, State<G>& s) {
#pragma unroll
    for (int i = 0; i < 4; ++i) {
        const f32x2 vf = {bflo(vv[i]), bfhi(vv[i])};
#pragma unroll
        for (int g = 0; g < G; ++g) { const f32x2 pp = {p[g], p[g]}; s.acc[g][jj * 4 + i] = __builtin_elementwise_fma(vf, pp, s.acc[g][jj * 4 + i]); }
    }
}
constexpr int REC = 260;
template <int G>
__device__ __forceinline__ void flush_state(State<G>& s, LAS float* rec0, int lane) {
#pragma unroll
    for (int g = 0; g < G; ++g) {
        const float M = xmax_groups(s.m[g]);
        const float wt = ex2(s.m[g] - M);
        const float L = xsum_groups(s.l[g] * wt);
        float v[32];
#pragma unroll
        for (int i = 0; i < 16; ++i) { v[2 * i] = s.acc[g][i][0] * wt; v[2 * i + 1] = s.acc[g][i][1] * wt; }
        float a[16], b8[8], c4[4];
#pragma unroll
        for (int i = 0; i < 16; ++i) a[i] = swap32_add(v[i], v[i + 16]);
#pragma unroll
        for (int i = 0; i < 8; ++i) b8[i] = swap16_add(a[i], a[i + 8]);
        const bool hi8 = (lane & 8) != 0;
#pragma unroll
        for (int i = 0; i < 4; ++i) { const float keep = hi8 ? b8[i + 4] : b8[i], send = hi8 ? b8[i] : b8[i + 4]; c4[i] = keep + dpp<ROR8>(send); }
        LAS float* rec = rec0 + g * REC;
        const int d0 = 64 * (lane >> 4) + 8 * (lane & 7) + 4 * ((lane >> 3) & 1);
        *(LAS f32x4*)(rec + d0) = f32x4{c4[0], c4[1], c4[2], c4[3]};
        if (lane == 0) { rec[256] = M; rec[257] = L; }
    }
}
template <int G>
__device__ __forceinline__ void state_init(State<G>& s) {
#pragma unroll
    for (int g = 0; g < G; ++g) { s.m[g] = -1e30f; s.l[g] = 0.f;
#pragma unroll
        for (int i = 0; i < 16; ++i) s.acc[g][i] = f32x2{0.f, 0.f}; }
}

constexpr int L_REC = 0;
constexpr int L_RED = WAVES * 3 * REC;
constexpr int L_OV = L_RED + 8 * 128;
constexpr int L_Q = L_OV + 128;
constexpr int L_FLAG = L_Q + 384;
constexpr int L_WO = L_FLAG + 4;
constexpr int L_END = L_WO + 1792 * 4;

__global__ void __launch_bounds__(NTHR, LB) k_fused(Params p) {
    __shared__ __attribute__((aligned(16))) float lds[L_END];
    LAS float* L = (LAS float*)lds;
    const int b = blockIdx.x, t = threadIdx.x, lane = t & 63, w = __builtin_amdgcn_readfirstlane(t >> 6);
    const int gA = b >> 5, iA = b & 31, hB = b >> 4, jB = b & 15;
    const int it = (b & 7) * 32 + (b >> 3);
    const rsrc_t rws = __builtin_amdgcn_make_buffer_rsrc((void*)p.ws, 0, WS_END, 0x00020000);
    STAMP_DECL;
    STAMP(0);
    {
        int off = -1; unsigned val = INVALID;
        if (t < 64) off = WS_PA + ((2 * gA) * 32 + iA) * 1024 + t * 16;
        else if (t < 128) off = WS_PA + ((2 * gA + 1) * 32 + iA) * 1024 + (t - 64) * 16;
        else if (t < 192) off = WS_PB + (hB * 16 + jB) * 1024 + (t - 128) * 16;
        else if (t < 220) off = WS_Y + ((((it >> 7) * 4 + (it & 3)) * 32 + ((it >> 2) & 31)) * 112) * 4 + (t - 192) * 16;
        else if (t < 222) off = WS_ML + ((2 * gA + (t - 220)) * 32 + iA) * 16;
        else if (t == 222) off = WS_MLB + (hB * 16 + jB) * 16;
        else if (t == 223 && b < 16) { off = WS_CTR + b * 256; val = 0u; }
        if (off >= 0) st128c(v4u{val, val, val, val}, rws, off);
    }
    SB();
    const int qpr = it >> 7, qcol0 = (it & 127) * 32;
    const int rg = t >> 2, ch = t & 3;
    v4u wv[7]; unsigned short xv[7];
    {
        const rsrc_t rwq = __builtin_amdgcn_make_buffer_rsrc((void*)p.wq[qpr], 0, DM * NQ * 2, 0x00020000);
        const rsrc_t rx = __builtin_amdgcn_make_buffer_rsrc((void*)p.x[qpr], 0, DM * 2, 0x00020000);
#pragma unroll
        for (int i = 0; i < 7; ++i) wv[i] = ldnt(rwq, rg * (NQ * 2) + qcol0 * 2 + ch * 16, i * (64 * NQ * 2));
#pragma unroll
        for (int i = 0; i < 7; ++i) xv[i] = __builtin_amdgcn_raw_buffer_load_b16(rx, rg * 2 + i * 128, 0, WQ_AUX);
    }
    v2u ep = {0u, 0u};
    if (w == 0) ep = ld64c(rws, WS_EPOCH);
    SB();
    const rsrc_t rkA = __builtin_amdgcn_make_buffer_rsrc((void*)p.k[0], 0, 8u * SK * DH * 2u, 0x00020000);
    const rsrc_t rvA = __builtin_amdgcn_make_buffer_rsrc((void*)p.v[0], 0, 8u * SK * DH * 2u, 0x00020000);
    const rsrc_t rkB = __builtin_amdgcn_make_buffer_rsrc((void*)p.k[1], 0, 16u * SK * DH * 2u, 0x00020000);
    const rsrc_t rvB = __builtin_amdgcn_make_buffer_rsrc((void*)p.v[1], 0, 16u * SK * DH * 2u, 0x00020000);
    const int vo = (lane >> 3) * 512 + (lane & 7) * 16;
#define PCX(AUX, r, o, j) __builtin_amdgcn_raw_buffer_load_b128(r, vo + ((j) & 3) * 128, (o) + ((j) >> 2) * 4096, AUX)
    constexpr unsigned strideA = PATA == 0 ? WAVES * TILE_B : 32 * WAVES * TILE_B, strideB = PATB == 0 ? WAVES * TILE_B : 16 * WAVES * TILE_B;
    const unsigned baseA = (unsigned)gA * (SK * 512u) + (unsigned)iA * (PATA == 0 ? NTA * WAVES * TILE_B : WAVES * TILE_B) + (unsigned)w * TILE_B;
    const unsigned headB = (unsigned)hB * (SK * 512u);
    const unsigned baseB = headB + (unsigned)jB * (PATB == 0 ? NSB * WAVES * TILE_B : WAVES * TILE_B) + (unsigned)w * TILE_B;
    v4u RK[8], RV[8];
    const int cpr = it >> 7, chq = (it >> 3) & 15, chf = (it >> 2) & 1, ccb = it & 3;
#pragma unroll
    for (int j = 0; j < 8; ++j) RK[j] = PCX(AUX_NT, rkA, baseA, j);
    SB();
    asm volatile("s_waitcnt vmcnt(8)" : "+v"(wv[0]), "+v"(wv[1]), "+v"(wv[2]), "+v"(wv[3]), "+v"(wv[4]), "+v"(wv[5]), "+v"(wv[6]),
                 "+v"(xv[0]), "+v"(xv[1]), "+v"(xv[2]), "+v"(xv[3]), "+v"(xv[4]), "+v"(xv[5]), "+v"(xv[6]), "+v"(ep)::"memory");
    STAMP(10);
    if (w != 0) {
#pragma unroll
        for (int j = 0; j < 8; ++j) RV[j] = PCX(AUX_NT, rvA, baseA, j);
    } else {
#pragma unroll
        for (int j = 0; j < 8; ++j) asm volatile("" : "=v"(RV[j]));
    }
    SB();
    {
        float acc[8];
#pragma unroll
        for (int e = 0; e < 8; ++e) acc[e] = 0.f;
#pragma unroll
        for (int i = 0; i < 7; ++i) {
            const float xf = asf((unsigned)xv[i] << 16);
#pragma unroll
            for (int e = 0; e < 4; ++e) { acc[2 * e] = fmaf(xf, bflo(wv[i][e]), acc[2 * e]); acc[2 * e + 1] = fmaf(xf, bfhi(wv[i][e]), acc[2 * e + 1]); }
        }
#pragma unroll
        for (int e = 0; e < 8; ++e) { float v = acc[e]; v += dpp<0x124>(v);   v += dpp<ROR8>(v); acc[e] = v; }
        float b4[4], c2[2];
#pragma unroll
        for (int e = 0; e < 4; ++e) b4[e] = swap16_add(acc[e], acc[e + 4]);
#pragma unroll
        for (int e = 0; e < 2; ++e) c2[e] = swap32_add(b4[e], b4[e + 2]);
        if ((lane & 12) == 0) *(LAS f32x2*)(L + L_RED + w * 32 + ch * 8 + 2 * (lane >> 5) + 4 * ((lane >> 4) & 1)) = f32x2{c2[0], c2[1]};
        if (t == 0) L[L_FLAG] = 0.f;
    }
    BARRIER();
    STAMP(11);
    u64 E = 0, tag = 0;
    if (w == 0) {
        E = ((u64)ep.y << 32) | ep.x;
        tag = (E + 1) & 0xffffffffffffull;
        if (lane < 32) {
            const float v = (L[L_RED + lane] + L[L_RED + 32 + lane]) + (L[L_RED + 64 + lane] + L[L_RED + 96 + lane]);
            const u64 g = (tag << 16) | (cvtpk(v, 0.f) & 0xffffu);
            st64c(v2u{(unsigned)g, (unsigned)(g >> 32)}, rws, WS_QG + (qpr * NQ + qcol0 + lane) * 8);
        } else if (lane == 32) {
            st64c(v2u{(unsigned)tag, (unsigned)(tag >> 32)}, rws, WS_READY + b * 8);
        }
        STAMP(1);
        const unsigned glo = (unsigned)tag << 16, ghi = (unsigned)(tag >> 16);
        v4u g[6];
        for (unsigned spins = 0;; ++spins) {
#pragma unroll
            for (int r = 0; r < 3; ++r) {
                const int row = r == 0 ? 2 * gA : r == 1 ? 2 * gA + 1 : 16 + hB;
                g[2 * r] = ld128c(rws, WS_QG + row * (DH * 8) + lane * 16);
                g[2 * r + 1] = ld128c(rws, WS_QG + row * (DH * 8) + 1024 + lane * 16);
            }
            bool ok = true;
#pragma unroll
            for (int r = 0; r < 6; ++r) ok = ok && (g[r].x & 0xffff0000u) == glo && g[r].y == ghi && (g[r].z & 0xffff0000u) == glo && g[r].w == ghi;
            if (all_lanes(ok) || spins > SPIN_CAP) break;
            __builtin_amdgcn_s_sleep(1);
        }
        LAS unsigned* qs = (LAS unsigned*)(L + L_Q);
#pragma unroll
        for (int r = 0; r < 6; ++r) qs[(r >> 1) * 128 + (r & 1) * 64 + lane] = (g[r].x & 0xffffu) | (g[r].z << 16);
        SB();
#pragma unroll
        for (int j = 0; j < 8; ++j) asm volatile("" ::"v"(RV[j]));
#pragma unroll
        for (int j = 0; j < 8; ++j) RV[j] = PCX(AUX_NT, rvA, baseA, j);
        SB();
    } else {
        const rsrc_t rwo = __builtin_amdgcn_make_buffer_rsrc((void*)p.wo[cpr], 0, DM * NQ * 2, 0x00020000);
        const int wob = ((chq * 256 + chf * 128) * DM + ccb * 112) * 2;
        v4u wo[10];
#pragma unroll
        for (int i = 0; i < 10; ++i) { const int e = t - 64 + 192 * i; const int ec = e < 1792 ? e : 0; wo[i] = ldnt(rwo, wob + (ec / 14) * (DM * 2) + (ec % 14) * 16, 0); }
#pragma unroll
        for (int i = 0; i < 10; ++i) { const int e = t - 64 + 192 * i; PIN(wo[i]); if (e < 1792) *(LAS v4u*)(L + L_WO + e * 4) = wo[i]; }
    }
    BARRIER();
    v4u qA[2][4];
    {
        const LAS unsigned char* qs = (const LAS unsigned char*)(L + L_Q);
#pragma unroll
        for (int jj = 0; jj < 4; ++jj) {
            const int d = 16 * ((lane & 7) + 8 * jj);
            qA[0][jj] = *(const LAS v4u*)(qs + d); qA[1][jj] = *(const LAS v4u*)(qs + 512 + d);
        }
    }
    GAS unsigned* ctr = (GAS unsigned*)(p.ws + WS_CTR) + hB * 64;
    asm volatile("" : "+v"(ctr));
    STAMP(2);
    {
        State<2> s; state_init(s);
        unsigned oc = baseA;
#pragma unroll 1
        for (int i = 0; i < NTA; ++i) {
            const bool last = (i == NTA - 1);
            const rsrc_t rkn = last ? rkB : rkA, rvn = last ? rvB : rvA;
            const unsigned on = last ? baseB : oc + strideA;
            float x0[2] = {0.f, 0.f}, x1[2] = {0.f, 0.f};
#pragma unroll
            for (int j = 0; j < 8; ++j) {
                if (j < 4) { qk_piece<2>(RK[j], qA, j & 3, x0); PIN(x0[0]); PIN(x0[1]); } else { qk_piece<2>(RK[j], qA, j & 3, x1); PIN(x1[0]); PIN(x1[1]); }
                RK[j] = PCX(AUX_KA, rkn, on, j);
                SB();
            }
            float p0[2], p1[2];
            softmax_step<2>(x0, x1, s, p0, p1);
            SB();
#pragma unroll
            for (int j = 0; j < 8; ++j) {
                if (j < 4) pv_piece<2>(RV[j], p0, j & 3, s); else pv_piece<2>(RV[j], p1, j & 3, s);
                PIN4(s.acc[0], (j & 3) * 4); PIN4(s.acc[1], (j & 3) * 4);
                RV[j] = PCX(AUX_VA, rvn, on, j);
                SB();
            }
            oc = on;
        }
        flush_state<2>(s, L + L_REC + (w * 3) * REC, lane);
    }
    STAMP(3);
    if (w == 0) {
        const unsigned tlo = (unsigned)tag, thi = (unsigned)(tag >> 32);
        for (unsigned spins = 0;; ++spins) {
            const v4u rd0 = ld128c(rws, WS_READY + lane * 32), rd1 = ld128c(rws, WS_READY + lane * 32 + 16);
            const bool ok = rd0.x == tlo && rd0.y == thi && rd0.z == tlo && rd0.w == thi && rd1.x == tlo && rd1.y == thi && rd1.z == tlo && rd1.w == thi;
            if (all_lanes(ok) || spins > SPIN_CAP) break;
            __builtin_amdgcn_s_sleep(2);
        }
        *(volatile LAS unsigned*)(L + L_FLAG) = 1u;
        if (b == 0 && lane == 0) { const u64 e1 = E + 1; st64c(v2u{(unsigned)e1, (unsigned)(e1 >> 32)}, rws, WS_EPOCH); }
    }
    {
        v4u qB[1][4];
        {
            const LAS unsigned char* qs = (const LAS unsigned char*)(L + L_Q);
#pragma unroll
            for (int jj = 0; jj < 4; ++jj) qB[0][jj] = *(const LAS v4u*)(qs + 1024 + 16 * ((lane & 7) + 8 * jj));
        }
        State<1> s; state_init(s);
        const rsrc_t rnull = __builtin_amdgcn_make_buffer_rsrc((void*)p.k[1], 0, 0, 0x00020000);
        unsigned on = baseB + strideB;
        int n = 1;
        bool have = true;
        bool more;
        do {
            unsigned tk = 0xffffffffu;
            if (NPOOL > 0 && n + 1 >= NSB && have) {
                if (n + 1 == NSB && w != 0) { for (unsigned spins = 0; *(volatile LAS unsigned*)(L + L_FLAG) == 0u && spins < (SPIN_CAP << 4); ++spins) __builtin_amdgcn_s_sleep(1); }
                if (lane == 0) tk = __hip_atomic_fetch_add(ctr, 1u, __ATOMIC_RELAXED, __HIP_MEMORY_SCOPE_AGENT);
            }
            const rsrc_t rk = have ? rkB : rnull, rv = have ? rvB : rnull;
            SB();
            float x0[1] = {0.f}, x1[1] = {0.f};
            float p0[1], p1[1];
#pragma unroll
            for (int j = 0; j < 8; ++j) {
                if (j < 4) { qk_piece<1>(RK[j], qB, j & 3, x0); PIN(x0[0]); } else { qk_piece<1>(RK[j], qB, j & 3, x1); PIN(x1[0]); }
                RK[j] = PCX(AUX_KB, rk, on, j);
                SB();
            }
            softmax_step<1>(x0, x1, s, p0, p1);
            SB();
#pragma unroll
            for (int j = 0; j < 8; ++j) {
                if (j < 4) pv_piece<1>(RV[j], p0, j & 3, s); else pv_piece<1>(RV[j], p1, j & 3, s);
                PIN4(s.acc[0], (j & 3) * 4);
                RV[j] = PCX(AUX_VB, rv, on, j);
                SB();
            }
            more = have;
            ++n;
            if (n < NSB) on += strideB;
            else { const unsigned tt = __builtin_amdgcn_readfirstlane(tk); have = tt < (unsigned)NPOOL; on = headB + (POOL0 + tt) * TILE_B; }
        } while (more);
        flush_state<1>(s, L + L_REC + (w * 3 + 2) * REC, lane);
    }
    STAMP(8);
    BARRIER();
    STAMP(4);
    if (t < 192) {
        const int hs = w, d0 = lane * 4;
        float M = -1e30f;
#pragma unroll
        for (int ww = 0; ww < WAVES; ++ww) M = fmaxf(M, L[L_REC + (ww * 3 + hs) * REC + 256]);
        f32x4 a = {0.f, 0.f, 0.f, 0.f}; float Ls = 0.f;
#pragma unroll
        for (int ww = 0; ww < WAVES; ++ww) {
            const LAS float* rec = L + L_REC + (ww * 3 + hs) * REC;
            const float wt = ex2(rec[256] - M);
            a += *(const LAS f32x4*)(rec + d0) * wt; Ls = fmaf(rec[257], wt, Ls);
        }
        int dst, ml;
        if (hs < 2) { const int hq = 2 * gA + hs; dst = WS_PA + (hq * 32 + iA) * 1024; ml = WS_ML + (hq * 32 + iA) * 16; }
        else { dst = WS_PB + (hB * 16 + jB) * 1024; ml = WS_MLB + (hB * 16 + jB) * 16; }
        st128c(__builtin_bit_cast(v4u, a), rws, dst + lane * 16);
        if (lane == 0) st64c(v2u{asu(M), asu(Ls)}, rws, ml);
    }
    {
        const int S8 = cpr == 0 ? 4 : 2;
        const int sg = lane >> 3, cl = lane & 7;
        const int rgp = lane >> 4, cc = (lane & 15) < 14 ? (lane & 15) : 13;
        v4u wr[8];
#pragma unroll
        for (int k = 0; k < 8; ++k) wr[k] = *(const LAS v4u*)(L + L_WO + ((32 * w + 8 * rgp + k) * 14 + cc) * 4);
        const int mlb = (cpr == 0 ? WS_ML : WS_MLB) + (chq * S8 * 8 + sg) * 16;
        const int pb = (cpr == 0 ? WS_PA : WS_PB) + (chq * S8 * 8 + sg) * 1024 + chf * 512 + (8 * w + cl) * 16;
        v2u ml[4]; v4u d[4];
        for (unsigned spins = 0;; ++spins) {
#pragma unroll
            for (int i = 0; i < 4; ++i) ml[i] = ld64c(rws, mlb + (i < S8 ? i : 0) * 128);
            bool ok = true;
#pragma unroll
            for (int i = 0; i < 4; ++i) ok = ok && ml[i].x != INVALID && ml[i].y != INVALID;
            if (all_lanes(ok) || spins > SPIN_CAP) break;
            __builtin_amdgcn_s_sleep(POLL_SLEEP);
        }
        for (unsigned spins = 0;; ++spins) {
#pragma unroll
            for (int i = 0; i < 4; ++i) d[i] = ld128c(rws, pb + (i < S8 ? i : 0) * 8192);
            bool ok = true;
#pragma unroll
            for (int i = 0; i < 4; ++i) ok = ok && valid4(d[i]);
            if (all_lanes(ok) || spins > SPIN_CAP) break;
            __builtin_amdgcn_s_sleep(1);
        }
        STAMP(5);
        float M = -1e30f;
#pragma unroll
        for (int i = 0; i < 4; ++i) if (i < S8) M = fmaxf(M, asf(ml[i].x));
        M = xmax_groups(M);
        float Lt = 0.f; f32x4 a = {0.f, 0.f, 0.f, 0.f};
#pragma unroll
        for (int i = 0; i < 4; ++i) if (i < S8) { const float wt = ex2(asf(ml[i].x) - M); Lt = fmaf(wt, asf(ml[i].y), Lt); a += __builtin_bit_cast(f32x4, d[i]) * wt; }
        Lt = xsum_groups(Lt);
        const float inv = 1.0f / Lt;
        f32x4 o4;
#pragma unroll
        for (int e = 0; e < 4; ++e) o4[e] = bflo(cvtpk(xsum_groups(a[e]) * inv, 0.f));
        if (lane < 8) *(LAS f32x4*)(L + L_OV + 32 * w + 4 * lane) = o4;
        asm volatile("s_waitcnt lgkmcnt(0)" ::: "memory");
        const f32x4 oa = *(const LAS f32x4*)(L + L_OV + 32 * w + 8 * rgp), ob = *(const LAS f32x4*)(L + L_OV + 32 * w + 8 * rgp + 4);
        float y[8];
#pragma unroll
        for (int e = 0; e < 8; ++e) y[e] = 0.f;
#pragma unroll
        for (int k = 0; k < 8; ++k) {
            const float o = k < 4 ? oa[k & 3] : ob[k & 3];
#pragma unroll
            for (int e = 0; e < 4; ++e) { y[2 * e] = fmaf(o, bflo(wr[k][e]), y[2 * e]); y[2 * e + 1] = fmaf(o, bfhi(wr[k][e]), y[2 * e + 1]); }
        }
        float b4[4], c2[2];
#pragma unroll
        for (int e = 0; e < 4; ++e) b4[e] = swap16_add(y[e], y[e + 4]);
#pragma unroll
        for (int e = 0; e < 2; ++e) c2[e] = swap32_add(b4[e], b4[e + 2]);
        if ((lane & 15) < 14) *(LAS f32x2*)(L + L_RED + w * 128 + cc * 8 + 2 * (lane >> 5) + 4 * ((lane >> 4) & 1)) = f32x2{c2[0], c2[1]};
    }
    BARRIER();
    if (t < 28) {
        const f32x4 v = (*(const LAS f32x4*)(L + L_RED + t * 4) + *(const LAS f32x4*)(L + L_RED + 128 + t * 4)) +
                        (*(const LAS f32x4*)(L + L_RED + 256 + t * 4) + *(const LAS f32x4*)(L + L_RED + 384 + t * 4));
        st128c(__builtin_bit_cast(v4u, v), rws, WS_Y + (((cpr * 4 + ccb) * 32 + chq * 2 + chf) * 112) * 4 + t * 16);
    }
    STAMP(6);
    if ((it & 7) == 0 && t < 224) {
        const int r = it >> 3, fpr = r >> 4, fcb = (r >> 2) & 3, sub = r & 3;
        const int c = t >> 5, s = t & 31;
        const int off = WS_Y + (((fpr * 4 + fcb) * 32 + s) * 112 + sub * 28 + c * 4) * 4;
        v4u d;
        for (unsigned spins = 0;; ++spins) {
            d = ld128c(rws, off);
            if (all_lanes(valid4(d)) || spins > SPIN_CAP) break;
            __builtin_amdgcn_s_sleep(1);
        }
        float v[4] = {asf(d.x), asf(d.y), asf(d.z), asf(d.w)};
#pragma unroll
        for (int e = 0; e < 4; ++e) { float z = v[e]; z += dpp<XOR1>(z); z += dpp<XOR2>(z); z += dpp<HMIRROR>(z); z += dpp<ROR8>(z); v[e] = x16_sum(z); }
        if (s == 0) *(GAS v2u*)(p.out + fpr * DM + fcb * 112 + sub * 28 + c * 4) = v2u{cvtpk(v[0], v[1]), cvtpk(v[2], v[3])};
    }
    STAMP(7);
    STAMP_FLUSH();
}
}

extern "C" void kernel_launch(void* const* d_in, const int* in_sizes, int n_in, void* d_out, int out_size, void* d_ws, size_t ws_size,
                              hipStream_t stream) {
    Params p;
    p.x[0] = (const unsigned short*)d_in[0]; p.k[0] = (const unsigned char*)d_in[1]; p.v[0] = (const unsigned char*)d_in[2];
    p.wq[0] = (const unsigned short*)d_in[3]; p.wo[0] = (const unsigned short*)d_in[4];
    p.x[1] = (const unsigned short*)d_in[5]; p.k[1] = (const unsigned char*)d_in[6]; p.v[1] = (const unsigned char*)d_in[7];
    p.wq[1] = (const unsigned short*)d_in[8]; p.wo[1] = (const unsigned short*)d_in[9];
    p.out = (unsigned short*)d_out; p.ws = (unsigned char*)d_ws;
    k_fused<<<NWG, NTHR, 0, stream>>>(p);
}
```

```cpp
#include <hip/hip_runtime.h>
#include <hip/hip_bf16.h>
#include <stdint.h>

#define GAS __attribute__((address_space(1)))
#define LAS __attribute__((address_space(3)))
typedef unsigned v4u __attribute__((ext_vector_type(4)));
typedef unsigned v2u __attribute__((ext_vector_type(2)));
typedef float f32x2 __attribute__((ext_vector_type(2)));
typedef float f32x4 __attribute__((ext_vector_type(4)));
typedef __bf16 bf16x2 __attribute__((ext_vector_type(2)));
typedef __amdgpu_buffer_rsrc_t rsrc_t;
typedef unsigned long long u64;

namespace {
constexpr int NWG = 256, WAVES = 4, NTHR = WAVES * 64;
constexpr int SK = 32768, DH = 256, DM = 448, NQ = 4096;
constexpr int TK = 16;
constexpr int TILE_B = TK * DH * 2;
constexpr int NTA = (SK / 32) / TK / WAVES;
constexpr int NTB_ALL = (SK / 16) / TK / WAVES;
#define LB 2
#define PREF 3
#define WQ_AUX 2
#define AUX_KA 2
#define AUX_VA 2
#define AUX_KB 2
#define AUX_VB 2
#define POLL_SLEEP 2
#define PATA 0
#define PATB 0
#define EPOCH_AUX 16
#define XPRE 3
#define NSB 20
constexpr int POOL0 = NSB * 16 * WAVES;
constexpr int NPOOL = (NTB_ALL - NSB) * 16 * WAVES;
#define NHALF 256
constexpr int NFULL = NPOOL - NHALF / 2;
constexpr int NTICK = NFULL + NHALF;
constexpr float SC = 0.0625f * 1.4426950408889634f;
constexpr unsigned INVALID = 0xffffffffu;
constexpr unsigned SPIN_CAP = 1u << 18;

constexpr unsigned WS_EPOCH = 0;
constexpr unsigned WS_READY = 4096;
constexpr unsigned WS_CTR = 8192;
constexpr unsigned WS_QG = 16384;
constexpr unsigned WS_ML = WS_QG + 65536;
constexpr unsigned WS_MLB = WS_ML + 16 * 32 * 16;
constexpr unsigned WS_PA = 131072;
constexpr unsigned WS_PB = WS_PA + 16 * 32 * 1024;
constexpr unsigned WS_Y = WS_PB + 16 * 16 * 1024;
constexpr unsigned WS_DIAG = WS_Y + 2 * 4 * 32 * 112 * 4;
constexpr unsigned WS_END = WS_DIAG + 256 * 16 * 8;

struct Params {
    const unsigned short* x[2];
    const unsigned char* k[2];
    const unsigned char* v[2];
    const unsigned short* wq[2];
    const unsigned short* wo[2];
    unsigned short* out;
    unsigned char* ws;
};

__device__ __forceinline__ float asf(unsigned u) { return __builtin_bit_cast(float, u); }
__device__ __forceinline__ unsigned asu(float f) { return __builtin_bit_cast(unsigned, f); }
__device__ __forceinline__ float dot2(unsigned a, unsigned b, float c) {
    return __builtin_amdgcn_fdot2_f32_bf16(__builtin_bit_cast(bf16x2, a), __builtin_bit_cast(bf16x2, b), c, false);
}
__device__ __forceinline__ unsigned cvtpk(float lo, float hi) {
    bf16x2 r = {(__bf16)lo, (__bf16)hi};
    return __builtin_bit_cast(unsigned, r);
}
template <int CTRL> __device__ __forceinline__ float dpp(float x) {
    return asf((unsigned)__builtin_amdgcn_mov_dpp((int)asu(x), CTRL, 0xf, 0xf, true));
}
constexpr int XOR1 = 0xB1, XOR2 = 0x4E, HMIRROR = 0x141, ROR8 = 0x128;
__device__ __forceinline__ float bflo(unsigned u) { return asf(u << 16); }
__device__ __forceinline__ float bfhi(unsigned u) { return asf(u & 0xffff0000u); }
__device__ __forceinline__ float ex2(float x) { return __builtin_amdgcn_exp2f(x); }
constexpr int AUX_NT = 2, AUX_SC1 = 16;
__device__ __forceinline__ v4u ldnt(rsrc_t r, int voff, unsigned soff) { return __builtin_amdgcn_raw_buffer_load_b128(r, voff, soff, AUX_NT); }
__device__ __forceinline__ v4u ld128c(rsrc_t r, int voff) { return __builtin_amdgcn_raw_buffer_load_b128(r, voff, 0, AUX_SC1); }
__device__ __forceinline__ v2u ld64c(rsrc_t r, int voff) { return __builtin_amdgcn_raw_buffer_load_b64(r, voff, 0, AUX_SC1); }
__device__ __forceinline__ void st128c(v4u v, rsrc_t r, int voff) { __builtin_amdgcn_raw_buffer_store_b128(v, r, voff, 0, AUX_SC1); }
__device__ __forceinline__ void st64c(v2u v, rsrc_t r, int voff) { __builtin_amdgcn_raw_buffer_store_b64(v, r, voff, 0, AUX_SC1); }
__device__ __forceinline__ void st32c(unsigned v, rsrc_t r, int voff) { __builtin_amdgcn_raw_buffer_store_b32(v, r, voff, 0, AUX_SC1); }
__device__ __forceinline__ float swap32_add(float a, float b) {
    auto r = __builtin_amdgcn_permlane32_swap(asu(a), asu(b), false, false);
    return asf(r[0]) + asf(r[1]);
}
__device__ __forceinline__ float swap16_add(float a, float b) {
    auto r = __builtin_amdgcn_permlane16_swap(asu(a), asu(b), false, false);
    return asf(r[0]) + asf(r[1]);
}
__device__ __forceinline__ float x16_max(float x) { auto s = __builtin_amdgcn_permlane16_swap(asu(x), asu(x), false, false); return fmaxf(asf(s[0]), asf(s[1])); }
__device__ __forceinline__ float x32_max(float x) { auto s = __builtin_amdgcn_permlane32_swap(asu(x), asu(x), false, false); return fmaxf(asf(s[0]), asf(s[1])); }
__device__ __forceinline__ float x16_sum(float x) { auto s = __builtin_amdgcn_permlane16_swap(asu(x), asu(x), false, false); return asf(s[0]) + asf(s[1]); }
__device__ __forceinline__ float x32_sum(float x) { auto s = __builtin_amdgcn_permlane32_swap(asu(x), asu(x), false, false); return asf(s[0]) + asf(s[1]); }
__device__ __forceinline__ float xmax_groups(float x) { x = fmaxf(x, dpp<ROR8>(x)); return x32_max(x16_max(x)); }
__device__ __forceinline__ float xsum_groups(float x) { x = x + dpp<ROR8>(x); return x32_sum(x16_sum(x)); }
__device__ __forceinline__ float wave_max(float x) { x = fmaxf(x, dpp<XOR1>(x)); x = fmaxf(x, dpp<XOR2>(x)); x = fmaxf(x, dpp<HMIRROR>(x)); return xmax_groups(x); }
__device__ __forceinline__ float wave_sum(float x) { x += dpp<XOR1>(x); x += dpp<XOR2>(x); x += dpp<HMIRROR>(x); return xsum_groups(x); }
__device__ __forceinline__ bool all_lanes(bool ok) { return __builtin_amdgcn_ballot_w64(ok) == __builtin_amdgcn_ballot_w64(true); }
__device__ __forceinline__ bool valid4(v4u v) { return v.x != INVALID && v.y != INVALID && v.z != INVALID && v.w != INVALID; }
#define PIN(x) asm volatile("" : "+v"(x)::"memory")
#define PIN4(a, o) asm volatile("" : "+v"((a)[(o)]), "+v"((a)[(o) + 1]), "+v"((a)[(o) + 2]), "+v"((a)[(o) + 3])::"memory")
#define SB() __builtin_amdgcn_sched_barrier(0)
#define BARRIER() do { asm volatile("s_waitcnt lgkmcnt(0)" ::: "memory"); __builtin_amdgcn_s_barrier(); asm volatile("" ::: "memory"); } while (0)
#define STAMP(i) do { } while (0)
#define STAMP_FLUSH() do { } while (0)
#define STAMP_DECL

template <int G> struct State { float m[G], l[G]; f32x2 acc[G][16]; };

template <int G>
__device__ __forceinline__ void qk_piece(const v4u& kv, const v4u (&q)[G][4], int jj, float (&x)[G]) {
#pragma unroll
    for (int g = 0; g < G; ++g) {
        float a = x[g];
#pragma unroll
        for (int i = 0; i < 4; ++i) a = dot2(kv[i], q[g][jj][i], a);
        x[g] = a;
    }
}
template <int G>
__device__ __forceinline__ void softmax_step(float (&x0)[G], float (&x1)[G], State<G>& s, float (&p0)[G], float (&p1)[G], bool half = false) {
#pragma unroll
    for (int g = 0; g < G; ++g) {
        float a = x0[g], b = x1[g];
        a += dpp<XOR1>(a); b += dpp<XOR1>(b);
        a += dpp<XOR2>(a); b += dpp<XOR2>(b);
        a += dpp<HMIRROR>(a); b += dpp<HMIRROR>(b);
        const unsigned pk = cvtpk(a, b);
        const float sa = bflo(pk) * SC, sb = half ? -1e30f : bfhi(pk) * SC;
        const float mn = fmaxf(s.m[g], fmaxf(sa, sb));
        const float al = ex2(s.m[g] - mn);
        p0[g] = ex2(sa - mn); p1[g] = ex2(sb - mn);
        s.l[g] = fmaf(s.l[g], al, p0[g] + p1[g]);
        s.m[g] = mn;
#pragma unroll
        for (int i = 0; i < 16; ++i) s.acc[g][i] *= al;
    }
}
template <int G>
__device__ __forceinline__ void pv_piece(const v4u& vv, const float (&p)[G], int jj, State<G>& s) {
#pragma unroll
    for (int i = 0; i < 4; ++i) {
        const f32x2 vf = {bflo(vv[i]), bfhi(vv[i])};
#pragma unroll
        for (int g = 0; g < G; ++g) { const f32x2 pp = {p[g], p[g]}; s.acc[g][jj * 4 + i] = __builtin_elementwise_fma(vf, pp, s.acc[g][jj * 4 + i]); }
    }
}
constexpr int REC = 260;
template <int G>
__device__ __forceinline__ void flush_state(State<G>& s, LAS float* rec0, int lane) {
#pragma unroll
    for (int g = 0; g < G; ++g) {
        const float M = xmax_groups(s.m[g]);
        const float wt = ex2(s.m[g] - M);
        const float L = xsum_groups(s.l[g] * wt);
        float v[32];
#pragma unroll
        for (int i = 0; i < 16; ++i) { v[2 * i] = s.acc[g][i][0] * wt; v[2 * i + 1] = s.acc[g][i][1] * wt; }
        float a[16], b8[8], c4[4];
#pragma unroll
        for (int i = 0; i < 16; ++i) a[i] = swap32_add(v[i], v[i + 16]);
#pragma unroll
        for (int i = 0; i < 8; ++i) b8[i] = swap16_add(a[i], a[i + 8]);
        const bool hi8 = (lane & 8) != 0;
#pragma unroll
        for (int i = 0; i < 4; ++i) { const float keep = hi8 ? b8[i + 4] : b8[i], send = hi8 ? b8[i] : b8[i + 4]; c4[i] = keep + dpp<ROR8>(send); }
        LAS float* rec = rec0 + g * REC;
        const int d0 = 64 * (lane >> 4) + 8 * (lane & 7) + 4 * ((lane >> 3) & 1);
        *(LAS f32x4*)(rec + d0) = f32x4{c4[0], c4[1], c4[2], c4[3]};
        if (lane == 0) { rec[256] = M; rec[257] = L; }
    }
}
template <int G>
__device__ __forceinline__ void state_init(State<G>& s) {
#pragma unroll
    for (int g = 0; g < G; ++g) { s.m[g] = -1e30f; s.l[g] = 0.f;
#pragma unroll
        for (int i = 0; i < 16; ++i) s.acc[g][i] = f32x2{0.f, 0.f}; }
}

constexpr int L_REC = 0;
constexpr int L_RED = WAVES * 3 * REC;
constexpr int L_OV = L_RED + 8 * 128;
constexpr int L_Q = L_OV + 128;
constexpr int L_FLAG = L_Q + 384;
constexpr int L_WO = L_FLAG + 4;
constexpr int L_END = L_WO + 1792 * 4;

__global__ void __launch_bounds__(NTHR, LB) k_fused(Params p) {
    __shared__ __attribute__((aligned(16))) float lds[L_END];
    LAS float* L = (LAS float*)lds;
    const int b = blockIdx.x, t = threadIdx.x, lane = t & 63, w = __builtin_amdgcn_readfirstlane(t >> 6);
    const int gA = b >> 5, iA = b & 31, hB = b >> 4, jB = b & 15;
    const int it = (b & 7) * 32 + (b >> 3);
    const rsrc_t rws = __builtin_amdgcn_make_buffer_rsrc((void*)p.ws, 0, WS_END, 0x00020000);
    STAMP_DECL;
    STAMP(0);
    {
        int off = -1; unsigned val = INVALID;
        if (t < 64) off = WS_PA + ((2 * gA) * 32 + iA) * 1024 + t * 16;
        else if (t < 128) off = WS_PA + ((2 * gA + 1) * 32 + iA) * 1024 + (t - 64) * 16;
        else if (t < 192) off = WS_PB + (hB * 16 + jB) * 1024 + (t - 128) * 16;
        else if (t < 220) off = WS_Y + ((((it >> 7) * 4 + (it & 3)) * 32 + ((it >> 2) & 31)) * 112) * 4 + (t - 192) * 16;
        else if (t < 222) off = WS_ML + ((2 * gA + (t - 220)) * 32 + iA) * 16;
        else if (t == 222) off = WS_MLB + (hB * 16 + jB) * 16;
        else if (t == 223 && b < 16) { off = WS_CTR + b * 256; val = 0u; }
        if (off >= 0) st128c(v4u{val, val, val, val}, rws, off);
    }
    SB();
    const int qpr = it >> 7, qcol0 = (it & 127) * 32;
    const int rg = t >> 2, ch = t & 3;
    v4u wv[7]; unsigned short xv[7];
    {
        const rsrc_t rwq = __builtin_amdgcn_make_buffer_rsrc((void*)p.wq[qpr], 0, DM * NQ * 2, 0x00020000);
        const rsrc_t rx = __builtin_amdgcn_make_buffer_rsrc((void*)p.x[qpr], 0, DM * 2, 0x00020000);
#pragma unroll
        for (int i = 0; i < 7; ++i) wv[i] = ldnt(rwq, rg * (NQ * 2) + qcol0 * 2 + ch * 16, i * (64 * NQ * 2));
#pragma unroll
        for (int i = 0; i < 7; ++i) xv[i] = __builtin_amdgcn_raw_buffer_load_b16(rx, rg * 2 + i * 128, 0, WQ_AUX);
    }
    v2u ep = {0u, 0u};
    if (w == 0) ep = ld64c(rws, WS_EPOCH);
    SB();
    const rsrc_t rkA = __builtin_amdgcn_make_buffer_rsrc((void*)p.k[0], 0, 8u * SK * DH * 2u, 0x00020000);
    const rsrc_t rvA = __builtin_amdgcn_make_buffer_rsrc((void*)p.v[0], 0, 8u * SK * DH * 2u, 0x00020000);
    const rsrc_t rkB = __builtin_amdgcn_make_buffer_rsrc((void*)p.k[1], 0, 16u * SK * DH * 2u, 0x00020000);
    const rsrc_t rvB = __builtin_amdgcn_make_buffer_rsrc((void*)p.v[1], 0, 16u * SK * DH * 2u, 0x00020000);
    const int vo = (lane >> 3) * 512 + (lane & 7) * 16;
#define PCX(AUX, r, o, j) __builtin_amdgcn_raw_buffer_load_b128(r, vo + ((j) & 3) * 128, (o) + ((j) >> 2) * 4096, AUX)
    constexpr unsigned strideA = PATA == 0 ? WAVES * TILE_B : 32 * WAVES * TILE_B, strideB = PATB == 0 ? WAVES * TILE_B : 16 * WAVES * TILE_B;
    const unsigned baseA = (unsigned)gA * (SK * 512u) + (unsigned)iA * (PATA == 0 ? NTA * WAVES * TILE_B : WAVES * TILE_B) + (unsigned)w * TILE_B;
    const unsigned headB = (unsigned)hB * (SK * 512u);
    const unsigned baseB = headB + (unsigned)jB * (PATB == 0 ? NSB * WAVES * TILE_B : WAVES * TILE_B) + (unsigned)w * TILE_B;
    v4u RK[8], RV[8];
    v4u RX[8];
    const int cpr = it >> 7, chq = (it >> 3) & 15, chf = (it >> 2) & 1, ccb = it & 3;
#pragma unroll
    for (int j = 0; j < 8; ++j) RK[j] = PCX(AUX_NT, rkA, baseA, j);
    SB();
    asm volatile("s_waitcnt vmcnt(8)" : "+v"(wv[0]), "+v"(wv[1]), "+v"(wv[2]), "+v"(wv[3]), "+v"(wv[4]), "+v"(wv[5]), "+v"(wv[6]),
                 "+v"(xv[0]), "+v"(xv[1]), "+v"(xv[2]), "+v"(xv[3]), "+v"(xv[4]), "+v"(xv[5]), "+v"(xv[6]), "+v"(ep)::"memory");
    STAMP(10);
    if (w != 0) {
#pragma unroll
        for (int j = 0; j < 8; ++j) RV[j] = PCX(AUX_NT, rvA, baseA, j);
    } else {
#pragma unroll
        for (int j = 0; j < 8; ++j) asm volatile("" : "=v"(RV[j]));
    }
    SB();
    {
        float acc[8];
#pragma unroll
        for (int e = 0; e < 8; ++e) acc[e] = 0.f;
#pragma unroll
        for (int i = 0; i < 7; ++i) {
            const float xf = asf((unsigned)xv[i] << 16);
#pragma unroll
            for (int e = 0; e < 4; ++e) { acc[2 * e] = fmaf(xf, bflo(wv[i][e]), acc[2 * e]); acc[2 * e + 1] = fmaf(xf, bfhi(wv[i][e]), acc[2 * e + 1]); }
        }
#pragma unroll
        for (int e = 0; e < 8; ++e) { float v = acc[e]; v += dpp<0x124>(v);   v += dpp<ROR8>(v); acc[e] = v; }
        float b4[4], c2[2];
#pragma unroll
        for (int e = 0; e < 4; ++e) b4[e] = swap16_add(acc[e], acc[e + 4]);
#pragma unroll
        for (int e = 0; e < 2; ++e) c2[e] = swap32_add(b4[e], b4[e + 2]);
        if ((lane & 12) == 0) *(LAS f32x2*)(L + L_RED + w * 32 + ch * 8 + 2 * (lane >> 5) + 4 * ((lane >> 4) & 1)) = f32x2{c2[0], c2[1]};
        if (t == 0) L[L_FLAG] = 0.f;
    }
    BARRIER();
    STAMP(11);
    u64 E = 0, tag = 0;
    if (w == 0) {
        E = ((u64)ep.y << 32) | ep.x;
        tag = (E + 1) & 0xffffffffffffull;
        if (lane < 32) {
            const float v = (L[L_RED + lane] + L[L_RED + 32 + lane]) + (L[L_RED + 64 + lane] + L[L_RED + 96 + lane]);
            const u64 g = (tag << 16) | (cvtpk(v, 0.f) & 0xffffu);
            st64c(v2u{(unsigned)g, (unsigned)(g >> 32)}, rws, WS_QG + (qpr * NQ + qcol0 + lane) * 8);
        } else if (lane == 32) {
            st64c(v2u{(unsigned)tag, (unsigned)(tag >> 32)}, rws, WS_READY + b * 8);
        }
        STAMP(1);
        const unsigned glo = (unsigned)tag << 16, ghi = (unsigned)(tag >> 16);
        v4u g[6];
        for (unsigned spins = 0;; ++spins) {
#pragma unroll
            for (int r = 0; r < 3; ++r) {
                const int row = r == 0 ? 2 * gA : r == 1 ? 2 * gA + 1 : 16 + hB;
                g[2 * r] = ld128c(rws, WS_QG + row * (DH * 8) + lane * 16);
                g[2 * r + 1] = ld128c(rws, WS_QG + row * (DH * 8) + 1024 + lane * 16);
            }
            bool ok = true;
#pragma unroll
            for (int r = 0; r < 6; ++r) ok = ok && (g[r].x & 0xffff0000u) == glo && g[r].y == ghi && (g[r].z & 0xffff0000u) == glo && g[r].w == ghi;
            if (all_lanes(ok) || spins > SPIN_CAP) break;
            __builtin_amdgcn_s_sleep(1);
        }
        LAS unsigned* qs = (LAS unsigned*)(L + L_Q);
#pragma unroll
        for (int r = 0; r < 6; ++r) qs[(r >> 1) * 128 + (r & 1) * 64 + lane] = (g[r].x & 0xffffu) | (g[r].z << 16);
        SB();
#pragma unroll
        for (int j = 0; j < 8; ++j) asm volatile("" ::"v"(RV[j]));
#pragma unroll
        for (int j = 0; j < 8; ++j) RV[j] = PCX(AUX_NT, rvA, baseA, j);
#pragma unroll
        for (int j = 0; j < 8; ++j) RX[j] = PCX(AUX_NT, rkA, baseA + strideA, j);
        SB();
    } else {
        const rsrc_t rwo = __builtin_amdgcn_make_buffer_rsrc((void*)p.wo[cpr], 0, DM * NQ * 2, 0x00020000);
        const int wob = ((chq * 256 + chf * 128) * DM + ccb * 112) * 2;
        v4u wo[10];
#pragma unroll
        for (int i = 0; i < 10; ++i) { const int e = t - 64 + 192 * i; const int ec = e < 1792 ? e : 0; wo[i] = ldnt(rwo, wob + (ec / 14) * (DM * 2) + (ec % 14) * 16, 0); }
#pragma unroll
        for (int i = 0; i < 10; ++i) { const int e = t - 64 + 192 * i; PIN(wo[i]); if (e < 1792) *(LAS v4u*)(L + L_WO + e * 4) = wo[i]; }
        SB();
#pragma unroll
        for (int j = 0; j < 8; ++j) RX[j] = PCX(AUX_NT, rkA, baseA + strideA, j);
        SB();
    }
    BARRIER();
    v4u qA[2][4];
    {
        const LAS unsigned char* qs = (const LAS unsigned char*)(L + L_Q);
#pragma unroll
        for (int jj = 0; jj < 4; ++jj) {
            const int d = 16 * ((lane & 7) + 8 * jj);
            qA[0][jj] = *(const LAS v4u*)(qs + d); qA[1][jj] = *(const LAS v4u*)(qs + 512 + d);
        }
    }
    GAS unsigned* ctr = (GAS unsigned*)(p.ws + WS_CTR) + hB * 64;
    asm volatile("" : "+v"(ctr));
    STAMP(2);
    {
        State<2> s; state_init(s);
        unsigned oc = baseA;
        int i = 0;
        {
            float x0[2] = {0.f, 0.f}, x1[2] = {0.f, 0.f};
#pragma unroll
            for (int j = 0; j < 8; ++j) {
                if (j < 4) { qk_piece<2>(RK[j], qA, j & 3, x0); PIN(x0[0]); PIN(x0[1]); } else { qk_piece<2>(RK[j], qA, j & 3, x1); PIN(x1[0]); PIN(x1[1]); }
                RK[j] = RX[j]; PIN(RK[j]);
                SB();
            }
            oc += strideA;
            float p0[2], p1[2];
            softmax_step<2>(x0, x1, s, p0, p1);
            SB();
#pragma unroll
            for (int j = 0; j < 8; ++j) {
                if (j < 4) pv_piece<2>(RV[j], p0, j & 3, s); else pv_piece<2>(RV[j], p1, j & 3, s);
                PIN4(s.acc[0], (j & 3) * 4); PIN4(s.acc[1], (j & 3) * 4);
                RV[j] = PCX(AUX_VA, rvA, oc, j);
                SB();
            }
            i = 1;
        }
#pragma unroll 1
        for (; i < NTA; ++i) {
            const bool last = (i == NTA - 1);
            const rsrc_t rkn = last ? rkB : rkA, rvn = last ? rvB : rvA;
            const unsigned on = last ? baseB : oc + strideA;
            float x0[2] = {0.f, 0.f}, x1[2] = {0.f, 0.f};
#pragma unroll
            for (int j = 0; j < 8; ++j) {
                if (j < 4) { qk_piece<2>(RK[j], qA, j & 3, x0); PIN(x0[0]); PIN(x0[1]); } else { qk_piece<2>(RK[j], qA, j & 3, x1); PIN(x1[0]); PIN(x1[1]); }
                RK[j] = PCX(AUX_KA, rkn, on, j);
                SB();
            }
            float p0[2], p1[2];
            softmax_step<2>(x0, x1, s, p0, p1);
            SB();
#pragma unroll
            for (int j = 0; j < 8; ++j) {
                if (j < 4) pv_piece<2>(RV[j], p0, j & 3, s); else pv_piece<2>(RV[j], p1, j & 3, s);
                PIN4(s.acc[0], (j & 3) * 4); PIN4(s.acc[1], (j & 3) * 4);
                RV[j] = PCX(AUX_VA, rvn, on, j);
                SB();
            }
            oc = on;
        }
        flush_state<2>(s, L + L_REC + (w * 3) * REC, lane);
    }
    STAMP(3);
    if (w == 0) {
        const unsigned tlo = (unsigned)tag, thi = (unsigned)(tag >> 32);
        for (unsigned spins = 0;; ++spins) {
            const v4u rd0 = ld128c(rws, WS_READY + lane * 32), rd1 = ld128c(rws, WS_READY + lane * 32 + 16);
            const bool ok = rd0.x == tlo && rd0.y == thi && rd0.z == tlo && rd0.w == thi && rd1.x == tlo && rd1.y == thi && rd1.z == tlo && rd1.w == thi;
            if (all_lanes(ok) || spins > SPIN_CAP) break;
            __builtin_amdgcn_s_sleep(2);
        }
        *(volatile LAS unsigned*)(L + L_FLAG) = 1u;
        if (b == 0 && lane == 0) { const u64 e1 = E + 1; st64c(v2u{(unsigned)e1, (unsigned)(e1 >> 32)}, rws, WS_EPOCH); }
    }
    {
        v4u qB[1][4];
        {
            const LAS unsigned char* qs = (const LAS unsigned char*)(L + L_Q);
#pragma unroll
            for (int jj = 0; jj < 4; ++jj) qB[0][jj] = *(const LAS v4u*)(qs + 1024 + 16 * ((lane & 7) + 8 * jj));
        }
        State<1> s; state_init(s);
        const rsrc_t rnull = __builtin_amdgcn_make_buffer_rsrc((void*)p.k[1], 0, 0, 0x00020000);
        unsigned on = baseB + strideB;
        int n = 1;
        bool have = true;
        bool onhalf = false, curhalf = false;
        bool more;
        do {
            unsigned tk = 0xffffffffu;
            if (NPOOL > 0 && n + 1 >= NSB && have) {
                if (n + 1 == NSB && w != 0) { for (unsigned spins = 0; *(volatile LAS unsigned*)(L + L_FLAG) == 0u && spins < (SPIN_CAP << 4); ++spins) __builtin_amdgcn_s_sleep(1); }
                if (lane == 0) tk = __hip_atomic_fetch_add(ctr, 1u, __ATOMIC_RELAXED, __HIP_MEMORY_SCOPE_AGENT);
            }
            const rsrc_t rk = have ? rkB : rnull, rv = have ? rvB : rnull;
            const rsrc_t rk2 = (have && !onhalf) ? rkB : rnull, rv2 = (have && !onhalf) ? rvB : rnull;
            SB();
            float x0[1] = {0.f}, x1[1] = {0.f};
            float p0[1], p1[1];
#pragma unroll
            for (int j = 0; j < 8; ++j) {
                if (j < 4) { qk_piece<1>(RK[j], qB, j & 3, x0); PIN(x0[0]); } else { qk_piece<1>(RK[j], qB, j & 3, x1); PIN(x1[0]); }
                RK[j] = PCX(AUX_KB, j < 4 ? rk : rk2, on, j);
                SB();
            }
            softmax_step<1>(x0, x1, s, p0, p1, curhalf);
            SB();
#pragma unroll
            for (int j = 0; j < 8; ++j) {
                if (j < 4) pv_piece<1>(RV[j], p0, j & 3, s); else pv_piece<1>(RV[j], p1, j & 3, s);
                PIN4(s.acc[0], (j & 3) * 4);
                RV[j] = PCX(AUX_VB, j < 4 ? rv : rv2, on, j);
                SB();
            }
            more = have;
            curhalf = onhalf;
            ++n;
            if (n < NSB) on += strideB;
            else {
                const unsigned tt = __builtin_amdgcn_readfirstlane(tk);
                have = tt < (unsigned)NTICK; onhalf = tt >= (unsigned)NFULL;
                on = headB + POOL0 * TILE_B + (onhalf ? NFULL * TILE_B + (tt - NFULL) * (TILE_B / 2) : tt * TILE_B);
            }
        } while (more);
        flush_state<1>(s, L + L_REC + (w * 3 + 2) * REC, lane);
    }
    STAMP(8);
    BARRIER();
    STAMP(4);
    if (t < 192) {
        const int hs = w, d0 = lane * 4;
        float M = -1e30f;
#pragma unroll
        for (int ww = 0; ww < WAVES; ++ww) M = fmaxf(M, L[L_REC + (ww * 3 + hs) * REC + 256]);
        f32x4 a = {0.f, 0.f, 0.f, 0.f}; float Ls = 0.f;
#pragma unroll
        for (int ww = 0; ww < WAVES; ++ww) {
            const LAS float* rec = L + L_REC + (ww * 3 + hs) * REC;
            const float wt = ex2(rec[256] - M);
            a += *(const LAS f32x4*)(rec + d0) * wt; Ls = fmaf(rec[257], wt, Ls);
        }
        int dst, ml;
        if (hs < 2) { const int hq = 2 * gA + hs; dst = WS_PA + (hq * 32 + iA) * 1024; ml = WS_ML + (hq * 32 + iA) * 16; }
        else { dst = WS_PB + (hB * 16 + jB) * 1024; ml = WS_MLB + (hB * 16 + jB) * 16; }
        st128c(__builtin_bit_cast(v4u, a), rws, dst + lane * 16);
        if (lane == 0) st64c(v2u{asu(M), asu(Ls)}, rws, ml);
    }
    {
        const int S8 = cpr == 0 ? 4 : 2;
        const int sg = lane >> 3, cl = lane & 7;
        const int rgp = lane >> 4, cc = (lane & 15) < 14 ? (lane & 15) : 13;
        v4u wr[8];
#pragma unroll
        for (int k = 0; k < 8; ++k) wr[k] = *(const LAS v4u*)(L + L_WO + ((32 * w + 8 * rgp + k) * 14 + cc) * 4);
        const int mlb = (cpr == 0 ? WS_ML : WS_MLB) + (chq * S8 * 8 + sg) * 16;
        const int pb = (cpr == 0 ? WS_PA : WS_PB) + (chq * S8 * 8 + sg) * 1024 + chf * 512 + (8 * w + cl) * 16;
        v2u ml[4]; v4u d[4];
        for (unsigned spins = 0;; ++spins) {
#pragma unroll
            for (int i = 0; i < 4; ++i) ml[i] = ld64c(rws, mlb + (i < S8 ? i : 0) * 128);
            bool ok = true;
#pragma unroll
            for (int i = 0; i < 4; ++i) ok = ok && ml[i].x != INVALID && ml[i].y != INVALID;
            if (all_lanes(ok) || spins > SPIN_CAP) break;
            __builtin_amdgcn_s_sleep(POLL_SLEEP);
        }
        for (unsigned spins = 0;; ++spins) {
#pragma unroll
            for (int i = 0; i < 4; ++i) d[i] = ld128c(rws, pb + (i < S8 ? i : 0) * 8192);
            bool ok = true;
#pragma unroll
            for (int i = 0; i < 4; ++i) ok = ok && valid4(d[i]);
            if (all_lanes(ok) || spins > SPIN_CAP) break;
            __builtin_amdgcn_s_sleep(1);
        }
        STAMP(5);
        float M = -1e30f;
#pragma unroll
        for (int i = 0; i < 4; ++i) if (i < S8) M = fmaxf(M, asf(ml[i].x));
        M = xmax_groups(M);
        float Lt = 0.f; f32x4 a = {0.f, 0.f, 0.f, 0.f};
#pragma unroll
        for (int i = 0; i < 4; ++i) if (i < S8) { const float wt = ex2(asf(ml[i].x) - M); Lt = fmaf(wt, asf(ml[i].y), Lt); a += __builtin_bit_cast(f32x4, d[i]) * wt; }
        Lt = xsum_groups(Lt);
        const float inv = 1.0f / Lt;
        f32x4 o4;
#pragma unroll
        for (int e = 0; e < 4; ++e) o4[e] = bflo(cvtpk(xsum_groups(a[e]) * inv, 0.f));
        if (lane < 8) *(LAS f32x4*)(L + L_OV + 32 * w + 4 * lane) = o4;
        asm volatile("s_waitcnt lgkmcnt(0)" ::: "memory");
        const f32x4 oa = *(const LAS f32x4*)(L + L_OV + 32 * w + 8 * rgp), ob = *(const LAS f32x4*)(L + L_OV + 32 * w + 8 * rgp + 4);
        float y[8];
#pragma unroll
        for (int e = 0; e < 8; ++e) y[e] = 0.f;
#pragma unroll
        for (int k = 0; k < 8; ++k) {
            const float o = k < 4 ? oa[k & 3] : ob[k & 3];
#pragma unroll
            for (int e = 0; e < 4; ++e) { y[2 * e] = fmaf(o, bflo(wr[k][e]), y[2 * e]); y[2 * e + 1] = fmaf(o, bfhi(wr[k][e]), y[2 * e + 1]); }
        }
        float b4[4], c2[2];
#pragma unroll
        for (int e = 0; e < 4; ++e) b4[e] = swap16_add(y[e], y[e + 4]);
#pragma unroll
        for (int e = 0; e < 2; ++e) c2[e] = swap32_add(b4[e], b4[e + 2]);
        if ((lane & 15) < 14) *(LAS f32x2*)(L + L_RED + w * 128 + cc * 8 + 2 * (lane >> 5) + 4 * ((lane >> 4) & 1)) = f32x2{c2[0], c2[1]};
    }
    BARRIER();
    if (t < 28) {
        const f32x4 v = (*(const LAS f32x4*)(L + L_RED + t * 4) + *(const LAS f32x4*)(L + L_RED + 128 + t * 4)) +
                        (*(const LAS f32x4*)(L + L_RED + 256 + t * 4) + *(const LAS f32x4*)(L + L_RED + 384 + t * 4));
        st128c(__builtin_bit_cast(v4u, v), rws, WS_Y + (((cpr * 4 + ccb) * 32 + chq * 2 + chf) * 112) * 4 + t * 16);
    }
    STAMP(6);
    if ((it & 7) == 0 && t < 224) {
        const int r = it >> 3, fpr = r >> 4, fcb = (r >> 2) & 3, sub = r & 3;
        const int c = t >> 5, s = t & 31;
        const int off = WS_Y + (((fpr * 4 + fcb) * 32 + s) * 112 + sub * 28 + c * 4) * 4;
        v4u d;
        for (unsigned spins = 0;; ++spins) {
            d = ld128c(rws, off);
            if (all_lanes(valid4(d)) || spins > SPIN_CAP) break;
            __builtin_amdgcn_s_sleep(1);
        }
        float v[4] = {asf(d.x), asf(d.y), asf(d.z), asf(d.w)};
#pragma unroll
        for (int e = 0; e < 4; ++e) { float z = v[e]; z += dpp<XOR1>(z); z += dpp<XOR2>(z); z += dpp<HMIRROR>(z); z += dpp<ROR8>(z); v[e] = x16_sum(z); }
        if (s == 0) *(GAS v2u*)(p.out + fpr * DM + fcb * 112 + sub * 28 + c * 4) = v2u{cvtpk(v[0], v[1]), cvtpk(v[2], v[3])};
    }
    STAMP(7);
    STAMP_FLUSH();
}
}

extern "C" void kernel_launch(void* const* d_in, const int* in_sizes, int n_in, void* d_out, int out_size, void* d_ws, size_t ws_size,
                              hipStream_t stream) {
    Params p;
    p.x[0] = (const unsigned short*)d_in[0]; p.k[0] = (const unsigned char*)d_in[1]; p.v[0] = (const unsigned char*)d_in[2];
    p.wq[0] = (const unsigned short*)d_in[3]; p.wo[0] = (const unsigned short*)d_in[4];
    p.x[1] = (const unsigned short*)d_in[5]; p.k[1] = (const unsigned char*)d_in[6]; p.v[1] = (const unsigned char*)d_in[7];
    p.wq[1] = (const unsigned short*)d_in[8]; p.wo[1] = (const unsigned short*)d_in[9];
    p.out = (unsigned short*)d_out; p.ws = (unsigned char*)d_ws;
    k_fused<<<NWG, NTHR, 0, stream>>>(p);
}
```

```cpp
#include <hip/hip_runtime.h>
#include <hip/hip_bf16.h>
#include <stdint.h>

#define GAS __attribute__((address_space(1)))
#define LAS __attribute__((address_space(3)))
typedef unsigned v4u __attribute__((ext_vector_type(4)));
typedef unsigned v2u __attribute__((ext_vector_type(2)));
typedef float f32x2 __attribute__((ext_vector_type(2)));
typedef float f32x4 __attribute__((ext_vector_type(4)));
typedef __bf16 bf16x2 __attribute__((ext_vector_type(2)));
typedef __amdgpu_buffer_rsrc_t rsrc_t;
typedef unsigned long long u64;

namespace {
constexpr int NWG = 256, WAVES = 4, NTHR = WAVES * 64;
constexpr int SK = 32768, DH = 256, DM = 448, NQ = 4096;
constexpr int TK = 16;
constexpr int TILE_B = TK * DH * 2;
constexpr int NTA = (SK / 32) / TK / WAVES;
constexpr int NTB_ALL = (SK / 16) / TK / WAVES;
#define NSB 18
#define NHALF 256
constexpr int POOL0 = NSB * 16 * WAVES;
constexpr int NPOOL = (NTB_ALL - NSB) * 16 * WAVES;
constexpr int NFULL = NPOOL - NHALF / 2;
constexpr int NTICK = NFULL + NHALF;
static_assert(NSB >= 2 && NSB < NTB_ALL && NHALF % 2 == 0 && NFULL >= 0, "pool shape");
constexpr float SC = 0.0625f * 1.4426950408889634f;
constexpr float M0 = -1e30f;
constexpr unsigned INVALID = 0xffffffffu;
constexpr unsigned SPIN_CAP = 1u << 18;

constexpr unsigned WS_EPOCH = 0;
constexpr unsigned WS_READY = 4096;
constexpr unsigned WS_CTR = 8192;
constexpr unsigned WS_QG = 16384;
constexpr unsigned WS_MLA = WS_QG + 65536;
constexpr unsigned WS_MLB = WS_MLA + 16 * 32 * 16;
constexpr unsigned WS_PA = 131072;
constexpr unsigned WS_PB = WS_PA + 16 * 32 * 1024;
constexpr unsigned WS_Y = WS_PB + 16 * 16 * 1024;
constexpr unsigned WS_DIAG = WS_Y + 2 * 4 * 32 * 112 * 4;
constexpr unsigned WS_END = WS_DIAG;

struct Params {
    const unsigned short* x[2];
    const unsigned char* k[2];
    const unsigned char* v[2];
    const unsigned short* wq[2];
    const unsigned short* wo[2];
    unsigned short* out;
    unsigned char* ws;
};

__device__ __forceinline__ float asf(unsigned u) { return __builtin_bit_cast(float, u); }
__device__ __forceinline__ unsigned asu(float f) { return __builtin_bit_cast(unsigned, f); }
__device__ __forceinline__ float dot2(unsigned a, unsigned b, float c) {
    return __builtin_amdgcn_fdot2_f32_bf16(__builtin_bit_cast(bf16x2, a), __builtin_bit_cast(bf16x2, b), c, false);
}
__device__ __forceinline__ unsigned cvtpk(float lo, float hi) {
    bf16x2 r = {(__bf16)lo, (__bf16)hi};
    return __builtin_bit_cast(unsigned, r);
}
template <int CTRL> __device__ __forceinline__ float dpp(float x) { return asf((unsigned)__builtin_amdgcn_mov_dpp((int)asu(x), CTRL, 0xf, 0xf, true)); }
constexpr int XOR1 = 0xB1, XOR2 = 0x4E, HMIRROR = 0x141, ROR4 = 0x124, ROR8 = 0x128;
__device__ __forceinline__ float bflo(unsigned u) { return asf(u << 16); }
__device__ __forceinline__ float bfhi(unsigned u) { return asf(u & 0xffff0000u); }
__device__ __forceinline__ float ex2(float x) { return __builtin_amdgcn_exp2f(x); }
constexpr int AUX_NT = 2, AUX_SC1 = 16, RSRC_W3 = 0x00020000;
__device__ __forceinline__ rsrc_t rsrc(const void* p, unsigned bytes) { return __builtin_amdgcn_make_buffer_rsrc((void*)p, 0, bytes, RSRC_W3); }
__device__ __forceinline__ v4u ldnt(rsrc_t r, int voff, unsigned soff) { return __builtin_amdgcn_raw_buffer_load_b128(r, voff, soff, AUX_NT); }
__device__ __forceinline__ v4u ld128c(rsrc_t r, int voff) { return __builtin_amdgcn_raw_buffer_load_b128(r, voff, 0, AUX_SC1); }
__device__ __forceinline__ v2u ld64c(rsrc_t r, int voff) { return __builtin_amdgcn_raw_buffer_load_b64(r, voff, 0, AUX_SC1); }
__device__ __forceinline__ void st128c(v4u v, rsrc_t r, int voff) { __builtin_amdgcn_raw_buffer_store_b128(v, r, voff, 0, AUX_SC1); }
__device__ __forceinline__ void st64c(v2u v, rsrc_t r, int voff) { __builtin_amdgcn_raw_buffer_store_b64(v, r, voff, 0, AUX_SC1); }
__device__ __forceinline__ void st64c(u64 v, rsrc_t r, int voff) { st64c(v2u{(unsigned)v, (unsigned)(v >> 32)}, r, voff); }
__device__ __forceinline__ float swap32_add(float a, float b) { auto r = __builtin_amdgcn_permlane32_swap(asu(a), asu(b), false, false); return asf(r[0]) + asf(r[1]); }
__device__ __forceinline__ float swap16_add(float a, float b) { auto r = __builtin_amdgcn_permlane16_swap(asu(a), asu(b), false, false); return asf(r[0]) + asf(r[1]); }
__device__ __forceinline__ float x16_max(float x) { auto s = __builtin_amdgcn_permlane16_swap(asu(x), asu(x), false, false); return fmaxf(asf(s[0]), asf(s[1])); }
__device__ __forceinline__ float x32_max(float x) { auto s = __builtin_amdgcn_permlane32_swap(asu(x), asu(x), false, false); return fmaxf(asf(s[0]), asf(s[1])); }
__device__ __forceinline__ float xmax_groups(float x) { x = fmaxf(x, dpp<ROR8>(x)); return x32_max(x16_max(x)); }
__device__ __forceinline__ float xsum_groups(float x) { x = x + dpp<ROR8>(x); x = swap16_add(x, x); return swap32_add(x, x); }
__device__ __forceinline__ bool all_lanes(bool ok) { return __builtin_amdgcn_ballot_w64(ok) == __builtin_amdgcn_ballot_w64(true); }
__device__ __forceinline__ unsigned umax4(v4u v) { return max(max(v.x, v.y), max(v.z, v.w)); }
__device__ __forceinline__ unsigned tagdiff(v4u g, unsigned lo, unsigned hi) { return ((g.x ^ lo) >> 16) | (g.y ^ hi) | ((g.z ^ lo) >> 16) | (g.w ^ hi); }
#define PIN(x) asm volatile("" : "+v"(x)::"memory")
#define PIN4(a, o) asm volatile("" : "+v"((a)[(o)]), "+v"((a)[(o) + 1]), "+v"((a)[(o) + 2]), "+v"((a)[(o) + 3])::"memory")
#define SB() __builtin_amdgcn_sched_barrier(0)
#define BARRIER() do { asm volatile("s_waitcnt lgkmcnt(0)" ::: "memory"); __builtin_amdgcn_s_barrier(); asm volatile("" ::: "memory"); } while (0)
#define DRAIN() asm volatile("s_waitcnt vmcnt(0)" ::: "memory")
#define STAMP(i) do { } while (0)
#define STAMP_FLUSH() do { } while (0)
#define STAMP_DECL

template <int G> struct State { float m[G], l[G]; f32x2 acc[G][16]; };

template <int G>
__device__ __forceinline__ void state_init(State<G>& s) {
#pragma unroll
    for (int g = 0; g < G; ++g) {
        s.m[g] = M0; s.l[g] = 0.f;
#pragma unroll
        for (int i = 0; i < 16; ++i) s.acc[g][i] = f32x2{0.f, 0.f};
    }
}
template <int G>
__device__ __forceinline__ void qk_piece(const v4u& kv, const v4u (&q)[G][4], int jj, float (&x)[G]) {
#pragma unroll
    for (int g = 0; g < G; ++g) {
        float a = x[g];
#pragma unroll
        for (int i = 0; i < 4; ++i) a = dot2(kv[i], q[g][jj][i], a);
        x[g] = a;
    }
}
template <int G>
__device__ __forceinline__ void softmax_step(float (&x0)[G], float (&x1)[G], State<G>& s, float (&p0)[G], float (&p1)[G], bool half) {
#pragma unroll
    for (int g = 0; g < G; ++g) {
        float a = x0[g], b = x1[g];
        a += dpp<XOR1>(a); b += dpp<XOR1>(b);
        a += dpp<XOR2>(a); b += dpp<XOR2>(b);
        a += dpp<HMIRROR>(a); b += dpp<HMIRROR>(b);
        const unsigned pk = cvtpk(a, b);
        const float sa = bflo(pk) * SC, sb = half ? M0 : bfhi(pk) * SC;
        const float mn = fmaxf(s.m[g], fmaxf(sa, sb));
        const float al = ex2(s.m[g] - mn);
        p0[g] = ex2(sa - mn); p1[g] = ex2(sb - mn);
        s.l[g] = fmaf(s.l[g], al, p0[g] + p1[g]);
        s.m[g] = mn;
#pragma unroll
        for (int i = 0; i < 16; ++i) s.acc[g][i] *= al;
    }
}
template <int G>
__device__ __forceinline__ void pv_piece(const v4u& vv, const float (&p)[G], int jj, State<G>& s) {
#pragma unroll
    for (int i = 0; i < 4; ++i) {
        const f32x2 vf = {bflo(vv[i]), bfhi(vv[i])};
#pragma unroll
        for (int g = 0; g < G; ++g) s.acc[g][jj * 4 + i] = __builtin_elementwise_fma(vf, f32x2{p[g], p[g]}, s.acc[g][jj * 4 + i]);
    }
}
constexpr int REC = 260;
template <int G>
__device__ __forceinline__ void flush_state(State<G>& s, LAS float* rec0, int lane) {
#pragma unroll
    for (int g = 0; g < G; ++g) {
        const float M = xmax_groups(s.m[g]);
        const float wt = ex2(s.m[g] - M);
        const float L = xsum_groups(s.l[g] * wt);
        float v[32], a[16], b8[8], c4[4];
#pragma unroll
        for (int i = 0; i < 16; ++i) { v[2 * i] = s.acc[g][i][0] * wt; v[2 * i + 1] = s.acc[g][i][1] * wt; }
#pragma unroll
        for (int i = 0; i < 16; ++i) a[i] = swap32_add(v[i], v[i + 16]);
#pragma unroll
        for (int i = 0; i < 8; ++i) b8[i] = swap16_add(a[i], a[i + 8]);
        const bool hi8 = (lane & 8) != 0;
#pragma unroll
        for (int i = 0; i < 4; ++i) { const float keep = hi8 ? b8[i + 4] : b8[i], send = hi8 ? b8[i] : b8[i + 4]; c4[i] = keep + dpp<ROR8>(send); }
        LAS float* rec = rec0 + g * REC;
        *(LAS f32x4*)(rec + 64 * (lane >> 4) + 8 * (lane & 7) + 4 * ((lane >> 3) & 1)) = f32x4{c4[0], c4[1], c4[2], c4[3]};
        if (lane == 0) { rec[256] = M; rec[257] = L; }
    }
}

constexpr int L_REC = 0;
constexpr int L_RED = WAVES * 3 * REC;
constexpr int L_OV = L_RED + WAVES * 128;
constexpr int L_Q = L_OV + 128;
constexpr int L_FLAG = L_Q + 384;
constexpr int L_WO = L_FLAG + 4;
constexpr int L_END = L_WO + 1792 * 4;

__global__ void __launch_bounds__(NTHR, 2) k_fused(Params p) {
    __shared__ __attribute__((aligned(16))) float lds[L_END];
    LAS float* L = (LAS float*)lds;
    const int b = blockIdx.x, t = threadIdx.x, lane = t & 63, w = __builtin_amdgcn_readfirstlane(t >> 6);
    const int gA = b >> 5, iA = b & 31;
    const int hB = b >> 4, jB = b & 15;
    const int it = (b & 7) * 32 + (b >> 3);
    STAMP_DECL;
    STAMP(0);

    const int qpr = it >> 7, qcol0 = (it & 127) * 32;
    const int rg = t >> 2, ch = t & 3;
    v4u wv[7]; unsigned short xv[7];
    {
        const rsrc_t rwq = rsrc(p.wq[qpr], DM * NQ * 2), rx = rsrc(p.x[qpr], DM * 2);
#pragma unroll
        for (int i = 0; i < 7; ++i) wv[i] = ldnt(rwq, rg * (NQ * 2) + qcol0 * 2 + ch * 16, i * (64 * NQ * 2));
#pragma unroll
        for (int i = 0; i < 7; ++i) xv[i] = __builtin_amdgcn_raw_buffer_load_b16(rx, rg * 2 + i * 128, 0, AUX_NT);
    }
    const rsrc_t rws = rsrc(p.ws, WS_END);
    v2u ep = {0u, 0u};
    if (w == 0) ep = ld64c(rws, WS_EPOCH);
    SB();
    const int cpr = it >> 7, chq = (it >> 3) & 15, chf = (it >> 2) & 1, ccb = it & 3;
    const int ybox = WS_Y + (((cpr * 4 + ccb) * 32 + chq * 2 + chf) * 112) * 4;
    int coff; unsigned cval = INVALID;
    if (w < 2) coff = WS_PA + ((2 * gA + w) * 32 + iA) * 1024 + lane * 16;
    else if (w == 2) coff = WS_PB + (hB * 16 + jB) * 1024 + lane * 16;
    else {
        coff = lane < 28 ? ybox + lane * 16 : lane < 30 ? WS_MLA + ((2 * gA + (lane - 28)) * 32 + iA) * 16 : lane == 30 ? WS_MLB + (hB * 16 + jB) * 16
             : (lane == 31 && b < 16) ? WS_CTR + b * 256 : (int)WS_END;
        if (lane == 31) cval = 0u;
    }
    PIN(coff); PIN(cval);
    SB();
    const rsrc_t rkA = rsrc(p.k[0], 8u * SK * DH * 2u), rvA = rsrc(p.v[0], 8u * SK * DH * 2u);
    const rsrc_t rkB = rsrc(p.k[1], 16u * SK * DH * 2u), rvB = rsrc(p.v[1], 16u * SK * DH * 2u);
    const int vo = (lane >> 3) * 512 + (lane & 7) * 16;
#define TILE_LD(r, o, j) __builtin_amdgcn_raw_buffer_load_b128(r, vo + ((j) & 3) * 128, (o) + ((j) >> 2) * 4096, AUX_NT)
    constexpr unsigned STRIDE = WAVES * TILE_B;
    const unsigned baseA = (unsigned)gA * (SK * 512u) + (unsigned)iA * (NTA * STRIDE) + (unsigned)w * TILE_B;
    const unsigned headB = (unsigned)hB * (SK * 512u);
    const unsigned baseB = headB + (unsigned)jB * (NSB * STRIDE) + (unsigned)w * TILE_B;
    v4u RK[8], RV[8], RX[8];
#pragma unroll
    for (int j = 0; j < 8; ++j) RK[j] = TILE_LD(rkA, baseA, j);
    SB();
    asm volatile("s_waitcnt vmcnt(8)" : "+v"(wv[0]), "+v"(wv[1]), "+v"(wv[2]), "+v"(wv[3]), "+v"(wv[4]), "+v"(wv[5]), "+v"(wv[6]),
                 "+v"(xv[0]), "+v"(xv[1]), "+v"(xv[2]), "+v"(xv[3]), "+v"(xv[4]), "+v"(xv[5]), "+v"(xv[6]), "+v"(ep)::"memory");
    STAMP(10);
    st128c(v4u{cval, cval, cval, cval}, rws, coff);
    if (w != 0) {
#pragma unroll
        for (int j = 0; j < 8; ++j) RV[j] = TILE_LD(rvA, baseA, j);
    } else {
#pragma unroll
        for (int j = 0; j < 8; ++j) asm volatile("" : "=v"(RV[j]));
    }
    SB();
    {
        float acc[8];
#pragma unroll
        for (int e = 0; e < 8; ++e) acc[e] = 0.f;
#pragma unroll
        for (int i = 0; i < 7; ++i) {
            const float xf = asf((unsigned)xv[i] << 16);
#pragma unroll
            for (int e = 0; e < 4; ++e) { acc[2 * e] = fmaf(xf, bflo(wv[i][e]), acc[2 * e]); acc[2 * e + 1] = fmaf(xf, bfhi(wv[i][e]), acc[2 * e + 1]); }
        }
#pragma unroll
        for (int e = 0; e < 8; ++e) { float v = acc[e]; v += dpp<ROR4>(v); v += dpp<ROR8>(v); acc[e] = v; }
        float b4[4], c2[2];
#pragma unroll
        for (int e = 0; e < 4; ++e) b4[e] = swap16_add(acc[e], acc[e + 4]);
#pragma unroll
        for (int e = 0; e < 2; ++e) c2[e] = swap32_add(b4[e], b4[e + 2]);
        if ((lane & 12) == 0) *(LAS f32x2*)(L + L_RED + w * 32 + ch * 8 + 2 * (lane >> 5) + 4 * ((lane >> 4) & 1)) = f32x2{c2[0], c2[1]};
        if (t == 0) { L[L_FLAG] = 0.f; L[L_FLAG + 1] = 0.f; }
    }
    BARRIER();
    STAMP(11);
    u64 epoch = 0, tag = 0;
    if (w == 0) {
        epoch = ((u64)ep.y << 32) | ep.x;
        tag = (epoch + 1) & 0xffffffffffffull;
        if (lane < 32) {
            const float v = (L[L_RED + lane] + L[L_RED + 32 + lane]) + (L[L_RED + 64 + lane] + L[L_RED + 96 + lane]);
            st64c((tag << 16) | (cvtpk(v, 0.f) & 0xffffu), rws, WS_QG + (qpr * NQ + qcol0 + lane) * 8);
        }
        STAMP(1);
        const unsigned glo = (unsigned)tag << 16, ghi = (unsigned)(tag >> 16);
        v4u g[4];
        for (unsigned spins = 0;; ++spins) {
            unsigned bad = 0;
#pragma unroll
            for (int r = 0; r < 4; ++r) g[r] = ld128c(rws, WS_QG + (2 * gA + (r >> 1)) * (DH * 8) + (r & 1) * 1024 + lane * 16);
#pragma unroll
            for (int r = 0; r < 4; ++r) bad |= tagdiff(g[r], glo, ghi);
            if (all_lanes(bad == 0) || spins > SPIN_CAP) break;
            __builtin_amdgcn_s_sleep(1);
        }
        LAS unsigned* qs = (LAS unsigned*)(L + L_Q);
#pragma unroll
        for (int r = 0; r < 4; ++r) qs[(r >> 1) * 128 + (r & 1) * 64 + lane] = (g[r].x & 0xffffu) | (g[r].z << 16);
        DRAIN();
        SB();
#pragma unroll
        for (int j = 0; j < 8; ++j) asm volatile("" ::"v"(RV[j]));
#pragma unroll
        for (int j = 0; j < 8; ++j) RV[j] = TILE_LD(rvA, baseA, j);
#pragma unroll
        for (int j = 0; j < 8; ++j) RX[j] = TILE_LD(rkA, baseA + STRIDE, j);
        SB();
    } else {
        const rsrc_t rwo = rsrc(p.wo[cpr], DM * NQ * 2);
        const int wob = ((chq * 256 + chf * 128) * DM + ccb * 112) * 2;
        v4u wo[10];
#pragma unroll
        for (int i = 0; i < 10; ++i) { const int e = t - 64 + 192 * i; const int ec = e < 1792 ? e : 0; wo[i] = ldnt(rwo, wob + (ec / 14) * (DM * 2) + (ec % 14) * 16, 0); }
#pragma unroll
        for (int i = 0; i < 10; ++i) { const int e = t - 64 + 192 * i; PIN(wo[i]); if (e < 1792) *(LAS v4u*)(L + L_WO + e * 4) = wo[i]; }
        DRAIN();
        SB();
#pragma unroll
        for (int j = 0; j < 8; ++j) RX[j] = TILE_LD(rkA, baseA + STRIDE, j);
        SB();
    }
    BARRIER();
    v4u qA[2][4];
    {
        const LAS unsigned char* qs = (const LAS unsigned char*)(L + L_Q);
#pragma unroll
        for (int jj = 0; jj < 4; ++jj) {
            const int d = 16 * ((lane & 7) + 8 * jj);
            qA[0][jj] = *(const LAS v4u*)(qs + d); qA[1][jj] = *(const LAS v4u*)(qs + 512 + d);
        }
    }
    if (w == 0) {
        if (lane == 0) st64c(tag, rws, WS_READY + b * 8);
        const unsigned glo = (unsigned)tag << 16, ghi = (unsigned)(tag >> 16);
        v4u g0, g1;
        for (unsigned spins = 0;; ++spins) {
            g0 = ld128c(rws, WS_QG + (16 + hB) * (DH * 8) + lane * 16);
            g1 = ld128c(rws, WS_QG + (16 + hB) * (DH * 8) + 1024 + lane * 16);
            if (all_lanes((tagdiff(g0, glo, ghi) | tagdiff(g1, glo, ghi)) == 0) || spins > SPIN_CAP) break;
            __builtin_amdgcn_s_sleep(1);
        }
        LAS unsigned* qs = (LAS unsigned*)(L + L_Q);
        qs[256 + lane] = (g0.x & 0xffffu) | (g0.z << 16);
        qs[320 + lane] = (g1.x & 0xffffu) | (g1.z << 16);
        asm volatile("s_waitcnt lgkmcnt(0)" ::: "memory");
        *(volatile LAS unsigned*)(L + L_FLAG + 1) = 1u;
    }
    STAMP(2);

    {
        State<2> s; state_init(s);
        unsigned oc = baseA + STRIDE;
        {
            float x0[2] = {0.f, 0.f}, x1[2] = {0.f, 0.f}, p0[2], p1[2];
#pragma unroll
            for (int j = 0; j < 8; ++j) {
                if (j < 4) { qk_piece<2>(RK[j], qA, j & 3, x0); PIN(x0[0]); PIN(x0[1]); } else { qk_piece<2>(RK[j], qA, j & 3, x1); PIN(x1[0]); PIN(x1[1]); }
                RK[j] = RX[j]; PIN(RK[j]);
                SB();
            }
            softmax_step<2>(x0, x1, s, p0, p1, false);
            SB();
#pragma unroll
            for (int j = 0; j < 8; ++j) {
                if (j < 4) pv_piece<2>(RV[j], p0, j & 3, s); else pv_piece<2>(RV[j], p1, j & 3, s);
                PIN4(s.acc[0], (j & 3) * 4); PIN4(s.acc[1], (j & 3) * 4);
                RV[j] = TILE_LD(rvA, oc, j);
                SB();
            }
        }
#pragma unroll 1
        for (int i = 1; i < NTA; ++i) {
            const bool last = (i == NTA - 1);
            const rsrc_t rkn = last ? rkB : rkA, rvn = last ? rvB : rvA;
            const unsigned on = last ? baseB : oc + STRIDE;
            float x0[2] = {0.f, 0.f}, x1[2] = {0.f, 0.f}, p0[2], p1[2];
#pragma unroll
            for (int j = 0; j < 8; ++j) {
                if (j < 4) { qk_piece<2>(RK[j], qA, j & 3, x0); PIN(x0[0]); PIN(x0[1]); } else { qk_piece<2>(RK[j], qA, j & 3, x1); PIN(x1[0]); PIN(x1[1]); }
                RK[j] = TILE_LD(rkn, on, j);
                SB();
            }
            softmax_step<2>(x0, x1, s, p0, p1, false);
            SB();
#pragma unroll
            for (int j = 0; j < 8; ++j) {
                if (j < 4) pv_piece<2>(RV[j], p0, j & 3, s); else pv_piece<2>(RV[j], p1, j & 3, s);
                PIN4(s.acc[0], (j & 3) * 4); PIN4(s.acc[1], (j & 3) * 4);
                RV[j] = TILE_LD(rvn, on, j);
                SB();
            }
            oc = on;
        }
        flush_state<2>(s, L + L_REC + (w * 3) * REC, lane);
    }
    STAMP(3);
    if (w == 0) {
        const unsigned tlo = (unsigned)tag, thi = (unsigned)(tag >> 32);
        for (unsigned spins = 0;; ++spins) {
            const v4u r0 = ld128c(rws, WS_READY + lane * 32), r1 = ld128c(rws, WS_READY + lane * 32 + 16);
            const unsigned bad = (r0.x ^ tlo) | (r0.y ^ thi) | (r0.z ^ tlo) | (r0.w ^ thi) | (r1.x ^ tlo) | (r1.y ^ thi) | (r1.z ^ tlo) | (r1.w ^ thi);
            if (all_lanes(bad == 0) || spins > SPIN_CAP) break;
            __builtin_amdgcn_s_sleep(2);
        }
        *(volatile LAS unsigned*)(L + L_FLAG) = 1u;
        if (b == 0 && lane == 0) st64c(epoch + 1, rws, WS_EPOCH);
    }
    const int rgp = lane >> 4, cc = (lane & 15) < 14 ? (lane & 15) : 13;
    f32x2 wf[8][4];
#pragma unroll
    for (int k = 0; k < 8; ++k) {
        const v4u wr = *(const LAS v4u*)(L + L_WO + ((32 * w + 8 * rgp + k) * 14 + cc) * 4);
#pragma unroll
        for (int e = 0; e < 4; ++e) wf[k][e] = f32x2{bflo(wr[e]), bfhi(wr[e])};
    }
#pragma unroll
    for (int k = 0; k < 8; ++k) asm volatile("" : "+v"(wf[k][0]), "+v"(wf[k][1]), "+v"(wf[k][2]), "+v"(wf[k][3]));

    {
        if (w != 0) { for (unsigned spins = 0; *(volatile LAS unsigned*)(L + L_FLAG + 1) == 0u && spins < (SPIN_CAP << 4); ++spins) __builtin_amdgcn_s_sleep(1); }
        asm volatile("" ::: "memory");
        v4u qB[1][4];
#pragma unroll
        for (int jj = 0; jj < 4; ++jj) qB[0][jj] = *(const LAS v4u*)((const LAS unsigned char*)(L + L_Q) + 1024 + 16 * ((lane & 7) + 8 * jj));
        GAS unsigned* ctr = (GAS unsigned*)(p.ws + WS_CTR) + hB * 64;
        asm volatile("" : "+v"(ctr));
        State<1> s; state_init(s);
        const rsrc_t rnull = rsrc(p.k[1], 0);
        unsigned on = baseB + STRIDE;
        int n = 1;
        bool have = true, onhalf = false, curhalf = false, more;
        do {
            unsigned tk = 0xffffffffu;
            if (n + 1 >= NSB && have) {
                if (n + 1 == NSB && w != 0) { for (unsigned spins = 0; *(volatile LAS unsigned*)(L + L_FLAG) == 0u && spins < (SPIN_CAP << 4); ++spins) __builtin_amdgcn_s_sleep(1); }
                if (lane == 0) tk = __hip_atomic_fetch_add(ctr, 1u, __ATOMIC_RELAXED, __HIP_MEMORY_SCOPE_AGENT);
            }
            const rsrc_t rk = have ? rkB : rnull, rv = have ? rvB : rnull;
            const rsrc_t rk2 = (have && !onhalf) ? rkB : rnull, rv2 = (have && !onhalf) ? rvB : rnull;
            SB();
            float x0[1] = {0.f}, x1[1] = {0.f}, p0[1], p1[1];
#pragma unroll
            for (int j = 0; j < 8; ++j) {
                if (j < 4) { qk_piece<1>(RK[j], qB, j & 3, x0); PIN(x0[0]); } else { qk_piece<1>(RK[j], qB, j & 3, x1); PIN(x1[0]); }
                RK[j] = TILE_LD(j < 4 ? rk : rk2, on, j);
                SB();
            }
            softmax_step<1>(x0, x1, s, p0, p1, curhalf);
            SB();
#pragma unroll
            for (int j = 0; j < 8; ++j) {
                if (j < 4) pv_piece<1>(RV[j], p0, j & 3, s); else pv_piece<1>(RV[j], p1, j & 3, s);
                PIN4(s.acc[0], (j & 3) * 4);
                RV[j] = TILE_LD(j < 4 ? rv : rv2, on, j);
                SB();
            }
            more = have;
            curhalf = onhalf;
            ++n;
            if (n < NSB) on += STRIDE;
            else {
                const unsigned tt = __builtin_amdgcn_readfirstlane(tk);
                have = tt < (unsigned)NTICK; onhalf = tt >= (unsigned)NFULL;
                on = headB + POOL0 * TILE_B + (onhalf ? NFULL * TILE_B + (tt - NFULL) * (TILE_B / 2) : tt * TILE_B);
            }
        } while (more);
        flush_state<1>(s, L + L_REC + (w * 3 + 2) * REC, lane);
    }
    STAMP(8);
    BARRIER();
    STAMP(4);

    if (w < 3) {
        float M = M0;
#pragma unroll
        for (int ww = 0; ww < WAVES; ++ww) M = fmaxf(M, L[L_REC + (ww * 3 + w) * REC + 256]);
        f32x4 a = {0.f, 0.f, 0.f, 0.f}; float Ls = 0.f;
#pragma unroll
        for (int ww = 0; ww < WAVES; ++ww) {
            const LAS float* rec = L + L_REC + (ww * 3 + w) * REC;
            const float wt = ex2(rec[256] - M);
            a += *(const LAS f32x4*)(rec + lane * 4) * wt; Ls = fmaf(rec[257], wt, Ls);
        }
        const int dst = w < 2 ? WS_PA + ((2 * gA + w) * 32 + iA) * 1024 : WS_PB + (hB * 16 + jB) * 1024;
        const int mlo = w < 2 ? WS_MLA + ((2 * gA + w) * 32 + iA) * 16 : WS_MLB + (hB * 16 + jB) * 16;
        st128c(__builtin_bit_cast(v4u, a), rws, dst + lane * 16);
        if (lane == 0) st64c(v2u{asu(M), asu(Ls)}, rws, mlo);
    }

    {
        const int S8 = cpr == 0 ? 4 : 2;
        const int sg = lane >> 3, cl = lane & 7;
        const int mlb = (cpr == 0 ? WS_MLA : WS_MLB) + (chq * S8 * 8 + sg) * 16;
        const int pb = (cpr == 0 ? WS_PA : WS_PB) + (chq * S8 * 8 + sg) * 1024 + chf * 512 + (8 * w + cl) * 16;
        v2u ml[4]; v4u d[4];
        for (unsigned spins = 0;; ++spins) {
            unsigned mx = 0;
#pragma unroll
            for (int i = 0; i < 4; ++i) ml[i] = ld64c(rws, mlb + (i < S8 ? i : 0) * 128);
#pragma unroll
            for (int i = 0; i < 4; ++i) mx = max(mx, max(ml[i].x, ml[i].y));
            if (all_lanes(mx != INVALID) || spins > SPIN_CAP) break;
            __builtin_amdgcn_s_sleep(2);
        }
        for (unsigned spins = 0;; ++spins) {
            unsigned mx = 0;
#pragma unroll
            for (int i = 0; i < 4; ++i) d[i] = ld128c(rws, pb + (i < S8 ? i : 0) * 8192);
#pragma unroll
            for (int i = 0; i < 4; ++i) mx = max(mx, umax4(d[i]));
            if (all_lanes(mx != INVALID) || spins > SPIN_CAP) break;
            __builtin_amdgcn_s_sleep(1);
        }
        STAMP(5);
        float M = M0;
#pragma unroll
        for (int i = 0; i < 4; ++i) if (i < S8) M = fmaxf(M, asf(ml[i].x));
        M = xmax_groups(M);
        float Lt = 0.f; f32x4 a = {0.f, 0.f, 0.f, 0.f};
#pragma unroll
        for (int i = 0; i < 4; ++i) if (i < S8) { const float wt = ex2(asf(ml[i].x) - M); Lt = fmaf(wt, asf(ml[i].y), Lt); a += __builtin_bit_cast(f32x4, d[i]) * wt; }
        const float inv = 1.0f / xsum_groups(Lt);
        f32x4 o4;
#pragma unroll
        for (int e = 0; e < 4; ++e) o4[e] = bflo(cvtpk(xsum_groups(a[e]) * inv, 0.f));
        if (lane < 8) *(LAS f32x4*)(L + L_OV + 32 * w + 4 * lane) = o4;
        asm volatile("s_waitcnt lgkmcnt(0)" ::: "memory");
        const f32x4 oa = *(const LAS f32x4*)(L + L_OV + 32 * w + 8 * rgp), ob = *(const LAS f32x4*)(L + L_OV + 32 * w + 8 * rgp + 4);
        f32x2 y2[4] = {f32x2{0.f, 0.f}, f32x2{0.f, 0.f}, f32x2{0.f, 0.f}, f32x2{0.f, 0.f}};
#pragma unroll
        for (int k = 0; k < 8; ++k) {
            const float o = k < 4 ? oa[k & 3] : ob[k & 3];
#pragma unroll
            for (int e = 0; e < 4; ++e) y2[e] = __builtin_elementwise_fma(f32x2{o, o}, wf[k][e], y2[e]);
        }
        float b4[4], c2[2];
#pragma unroll
        for (int e = 0; e < 4; ++e) b4[e] = swap16_add(y2[e >> 1][e & 1], y2[2 + (e >> 1)][e & 1]);
#pragma unroll
        for (int e = 0; e < 2; ++e) c2[e] = swap32_add(b4[e], b4[e + 2]);
        if ((lane & 15) < 14) *(LAS f32x2*)(L + L_RED + w * 128 + cc * 8 + 2 * (lane >> 5) + 4 * ((lane >> 4) & 1)) = f32x2{c2[0], c2[1]};
    }
    BARRIER();
    if (t < 28) {
        const f32x4 v = (*(const LAS f32x4*)(L + L_RED + t * 4) + *(const LAS f32x4*)(L + L_RED + 128 + t * 4)) +
                        (*(const LAS f32x4*)(L + L_RED + 256 + t * 4) + *(const LAS f32x4*)(L + L_RED + 384 + t * 4));
        st128c(__builtin_bit_cast(v4u, v), rws, ybox + t * 16);
    }
    STAMP(6);

    if ((it & 7) == 0 && t < 224) {
        const int r = it >> 3, fpr = r >> 4, fcb = (r >> 2) & 3, sub = r & 3;
        const int c = t >> 5, s = t & 31;
        const int off = WS_Y + (((fpr * 4 + fcb) * 32 + s) * 112 + sub * 28 + c * 4) * 4;
        v4u d;
        for (unsigned spins = 0;; ++spins) {
            d = ld128c(rws, off);
            if (all_lanes(umax4(d) != INVALID) || spins > SPIN_CAP) break;
            __builtin_amdgcn_s_sleep(1);
        }
        float v[4] = {asf(d.x), asf(d.y), asf(d.z), asf(d.w)};
#pragma unroll
        for (int e = 0; e < 4; ++e) { float z = v[e]; z += dpp<XOR1>(z); z += dpp<XOR2>(z); z += dpp<HMIRROR>(z); z += dpp<ROR8>(z); v[e] = swap16_add(z, z); }
        if (s == 0) st64c(v2u{cvtpk(v[0], v[1]), cvtpk(v[2], v[3])}, rsrc(p.out, 2 * DM * 2), (fpr * DM + fcb * 112 + sub * 28 + c * 4) * 2);
    }
    STAMP(7);
    STAMP_FLUSH();
}
}

extern "C" void kernel_launch(void* const* d_in, const int* in_sizes, int n_in, void* d_out, int out_size, void* d_ws, size_t ws_size,
                              hipStream_t stream) {
    Params p;
    p.x[0] = (const unsigned short*)d_in[0]; p.k[0] = (const unsigned char*)d_in[1]; p.v[0] = (const unsigned char*)d_in[2];
    p.wq[0] = (const unsigned short*)d_in[3]; p.wo[0] = (const unsigned short*)d_in[4];
    p.x[1] = (const unsigned short*)d_in[5]; p.k[1] = (const unsigned char*)d_in[6]; p.v[1] = (const unsigned char*)d_in[7];
    p.wq[1] = (const unsigned short*)d_in[8]; p.wo[1] = (const unsigned short*)d_in[9];
    p.out = (unsigned short*)d_out; p.ws = (unsigned char*)d_ws;
    k_fused<<<NWG, NTHR, 0, stream>>>(p);
}
```

```cpp
#include <hip/hip_runtime.h>
#include <hip/hip_bf16.h>
#include <stdint.h>

#define GAS __attribute__((address_space(1)))
#define LAS __attribute__((address_space(3)))
typedef unsigned v4u __attribute__((ext_vector_type(4)));
typedef unsigned v2u __attribute__((ext_vector_type(2)));
typedef float f32x2 __attribute__((ext_vector_type(2)));
typedef float f32x4 __attribute__((ext_vector_type(4)));
typedef __bf16 bf16x2 __attribute__((ext_vector_type(2)));
typedef __amdgpu_buffer_rsrc_t rsrc_t;
typedef unsigned long long u64;

namespace {
constexpr int NWG = 256, WAVES = 4, NTHR = WAVES * 64;
constexpr int SK = 32768, DH = 256, DM = 448, NQ = 4096;
constexpr int TK = 16;
constexpr int TILE_B = TK * DH * 2;
constexpr int NTA = (SK / 32) / TK / WAVES;
constexpr int NTB_ALL = (SK / 16) / TK / WAVES;
#define LB 2
#define PREF 3
#define WQ_AUX 2
#define AUX_KA 2
#define AUX_VA 2
#define AUX_KB 2
#define AUX_VB 2
#define POLL_SLEEP 2
#define PATA 0
#define PATB 0
#define EPOCH_AUX 16
#define XPRE 3
#define NSB 20
constexpr int POOL0 = NSB * 16 * WAVES;
constexpr int NPOOL = (NTB_ALL - NSB) * 16 * WAVES;
#define NHALF 256
constexpr int NFULL = NPOOL - NHALF / 2;
constexpr int NTICK = NFULL + NHALF;
constexpr float SC = 0.0625f * 1.4426950408889634f;
constexpr unsigned INVALID = 0xffffffffu;
constexpr unsigned SPIN_CAP = 1u << 18;

constexpr unsigned WS_EPOCH = 0;
constexpr unsigned WS_READY = 4096;
constexpr unsigned WS_CTR = 8192;
constexpr unsigned WS_QG = 16384;
constexpr unsigned WS_ML = WS_QG + 65536;
constexpr unsigned WS_MLB = WS_ML + 16 * 32 * 16;
constexpr unsigned WS_PA = 131072;
constexpr unsigned WS_PB = WS_PA + 16 * 32 * 1024;
constexpr unsigned WS_Y = WS_PB + 16 * 16 * 1024;
constexpr unsigned WS_DIAG = WS_Y + 2 * 4 * 32 * 112 * 4;
constexpr unsigned WS_END = WS_DIAG + 256 * 16 * 8;

struct Params {
    const unsigned short* x[2];
    const unsigned char* k[2];
    const unsigned char* v[2];
    const unsigned short* wq[2];
    const unsigned short* wo[2];
    unsigned short* out;
    unsigned char* ws;
};

__device__ __forceinline__ float asf(unsigned u) { return __builtin_bit_cast(float, u); }
__device__ __forceinline__ unsigned asu(float f) { return __builtin_bit_cast(unsigned, f); }
__device__ __forceinline__ float dot2(unsigned a, unsigned b, float c) {
    return __builtin_amdgcn_fdot2_f32_bf16(__builtin_bit_cast(bf16x2, a), __builtin_bit_cast(bf16x2, b), c, false);
}
__device__ __forceinline__ unsigned cvtpk(float lo, float hi) {
    bf16x2 r = {(__bf16)lo, (__bf16)hi};
    return __builtin_bit_cast(unsigned, r);
}
template <int CTRL> __device__ __forceinline__ float dpp(float x) {
    return asf((unsigned)__builtin_amdgcn_mov_dpp((int)asu(x), CTRL, 0xf, 0xf, true));
}
constexpr int XOR1 = 0xB1, XOR2 = 0x4E, HMIRROR = 0x141, ROR8 = 0x128;
__device__ __forceinline__ float bflo(unsigned u) { return asf(u << 16); }
__device__ __forceinline__ float bfhi(unsigned u) { return asf(u & 0xffff0000u); }
__device__ __forceinline__ float ex2(float x) { return __builtin_amdgcn_exp2f(x); }
constexpr int AUX_NT = 2, AUX_SC1 = 16;
__device__ __forceinline__ v4u ldnt(rsrc_t r, int voff, unsigned soff) { return __builtin_amdgcn_raw_buffer_load_b128(r, voff, soff, AUX_NT); }
__device__ __forceinline__ v4u ld128c(rsrc_t r, int voff) { return __builtin_amdgcn_raw_buffer_load_b128(r, voff, 0, AUX_SC1); }
__device__ __forceinline__ v2u ld64c(rsrc_t r, int voff) { return __builtin_amdgcn_raw_buffer_load_b64(r, voff, 0, AUX_SC1); }
__device__ __forceinline__ void st128c(v4u v, rsrc_t r, int voff) { __builtin_amdgcn_raw_buffer_store_b128(v, r, voff, 0, AUX_SC1); }
__device__ __forceinline__ void st64c(v2u v, rsrc_t r, int voff) { __builtin_amdgcn_raw_buffer_store_b64(v, r, voff, 0, AUX_SC1); }
__device__ __forceinline__ void st32c(unsigned v, rsrc_t r, int voff) { __builtin_amdgcn_raw_buffer_store_b32(v, r, voff, 0, AUX_SC1); }
__device__ __forceinline__ float swap32_add(float a, float b) {
    auto r = __builtin_amdgcn_permlane32_swap(asu(a), asu(b), false, false);
    return asf(r[0]) + asf(r[1]);
}
__device__ __forceinline__ float swap16_add(float a, float b) {
    auto r = __builtin_amdgcn_permlane16_swap(asu(a), asu(b), false, false);
    return asf(r[0]) + asf(r[1]);
}
__device__ __forceinline__ float x16_max(float x) { auto s = __builtin_amdgcn_permlane16_swap(asu(x), asu(x), false, false); return fmaxf(asf(s[0]), asf(s[1])); }
__device__ __forceinline__ float x32_max(float x) { auto s = __builtin_amdgcn_permlane32_swap(asu(x), asu(x), false, false); return fmaxf(asf(s[0]), asf(s[1])); }
__device__ __forceinline__ float x16_sum(float x) { auto s = __builtin_amdgcn_permlane16_swap(asu(x), asu(x), false, false); return asf(s[0]) + asf(s[1]); }
__device__ __forceinline__ float x32_sum(float x) { auto s = __builtin_amdgcn_permlane32_swap(asu(x), asu(x), false, false); return asf(s[0]) + asf(s[1]); }
__device__ __forceinline__ float xmax_groups(float x) { x = fmaxf(x, dpp<ROR8>(x)); return x32_max(x16_max(x)); }
__device__ __forceinline__ float xsum_groups(float x) { x = x + dpp<ROR8>(x); return x32_sum(x16_sum(x)); }
__device__ __forceinline__ float wave_max(float x) { x = fmaxf(x, dpp<XOR1>(x)); x = fmaxf(x, dpp<XOR2>(x)); x = fmaxf(x, dpp<HMIRROR>(x)); return xmax_groups(x); }
__device__ __forceinline__ float wave_sum(float x) { x += dpp<XOR1>(x); x += dpp<XOR2>(x); x += dpp<HMIRROR>(x); return xsum_groups(x); }
__device__ __forceinline__ bool all_lanes(bool ok) { return __builtin_amdgcn_ballot_w64(ok) == __builtin_amdgcn_ballot_w64(true); }
__device__ __forceinline__ unsigned umax4(v4u v) { return max(max(v.x, v.y), max(v.z, v.w)); }
__device__ __forceinline__ bool valid4(v4u v) { return umax4(v) != INVALID; }
#define PIN(x) asm volatile("" : "+v"(x)::"memory")
#define PIN4(a, o) asm volatile("" : "+v"((a)[(o)]), "+v"((a)[(o) + 1]), "+v"((a)[(o) + 2]), "+v"((a)[(o) + 3])::"memory")
#define SB() __builtin_amdgcn_sched_barrier(0)
#define BARRIER() do { asm volatile("s_waitcnt lgkmcnt(0)" ::: "memory"); __builtin_amdgcn_s_barrier(); asm volatile("" ::: "memory"); } while (0)
#define STAMP(i) do { } while (0)
#define STAMP_FLUSH() do { } while (0)
#define STAMP_DECL

template <int G> struct State { float m[G], l[G]; f32x2 acc[G][16]; };

template <int G>
__device__ __forceinline__ void qk_piece(const v4u& kv, const v4u (&q)[G][4], int jj, float (&x)[G]) {
#pragma unroll
    for (int g = 0; g < G; ++g) {
        float a = x[g];
#pragma unroll
        for (int i = 0; i < 4; ++i) a = dot2(kv[i], q[g][jj][i], a);
        x[g] = a;
    }
}
template <int G>
__device__ __forceinline__ void softmax_step(float (&x0)[G], float (&x1)[G], State<G>& s, float (&p0)[G], float (&p1)[G], bool half = false) {
#pragma unroll
    for (int g = 0; g < G; ++g) {
        float a = x0[g], b = x1[g];
        a += dpp<XOR1>(a); b += dpp<XOR1>(b);
        a += dpp<XOR2>(a); b += dpp<XOR2>(b);
        a += dpp<HMIRROR>(a); b += dpp<HMIRROR>(b);
        const unsigned pk = cvtpk(a, b);
        const float sa = bflo(pk) * SC, sb = half ? -1e30f : bfhi(pk) * SC;
        const float mn = fmaxf(s.m[g], fmaxf(sa, sb));
        const float al = ex2(s.m[g] - mn);
        p0[g] = ex2(sa - mn); p1[g] = ex2(sb - mn);
        s.l[g] = fmaf(s.l[g], al, p0[g] + p1[g]);
        s.m[g] = mn;
#pragma unroll
        for (int i = 0; i < 16; ++i) s.acc[g][i] *= al;
    }
}
template <int G>
__device__ __forceinline__ void pv_piece(const v4u& vv, const float (&p)[G], int jj, State<G>& s) {
#pragma unroll
    for (int i = 0; i < 4; ++i) {
        const f32x2 vf = {bflo(vv[i]), bfhi(vv[i])};
#pragma unroll
        for (int g = 0; g < G; ++g) { const f32x2 pp = {p[g], p[g]}; s.acc[g][jj * 4 + i] = __builtin_elementwise_fma(vf, pp, s.acc[g][jj * 4 + i]); }
    }
}
constexpr int REC = 260;
template <int G>
__device__ __forceinline__ void flush_state(State<G>& s, LAS float* rec0, int lane) {
#pragma unroll
    for (int g = 0; g < G; ++g) {
        const float M = xmax_groups(s.m[g]);
        const float wt = ex2(s.m[g] - M);
        const float L = xsum_groups(s.l[g] * wt);
        float v[32];
#pragma unroll
        for (int i = 0; i < 16; ++i) { v[2 * i] = s.acc[g][i][0] * wt; v[2 * i + 1] = s.acc[g][i][1] * wt; }
        float a[16], b8[8], c4[4];
#pragma unroll
        for (int i = 0; i < 16; ++i) a[i] = swap32_add(v[i], v[i + 16]);
#pragma unroll
        for (int i = 0; i < 8; ++i) b8[i] = swap16_add(a[i], a[i + 8]);
        const bool hi8 = (lane & 8) != 0;
#pragma unroll
        for (int i = 0; i < 4; ++i) { const float keep = hi8 ? b8[i + 4] : b8[i], send = hi8 ? b8[i] : b8[i + 4]; c4[i] = keep + dpp<ROR8>(send); }
        LAS float* rec = rec0 + g * REC;
        const int d0 = 64 * (lane >> 4) + 8 * (lane & 7) + 4 * ((lane >> 3) & 1);
        *(LAS f32x4*)(rec + d0) = f32x4{c4[0], c4[1], c4[2], c4[3]};
        if (lane == 0) { rec[256] = M; rec[257] = L; }
    }
}
template <int G>
__device__ __forceinline__ void state_init(State<G>& s) {
#pragma unroll
    for (int g = 0; g < G; ++g) { s.m[g] = -1e30f; s.l[g] = 0.f;
#pragma unroll
        for (int i = 0; i < 16; ++i) s.acc[g][i] = f32x2{0.f, 0.f}; }
}

constexpr int L_REC = 0;
constexpr int L_RED = WAVES * 3 * REC;
constexpr int L_OV = L_RED + 8 * 128;
constexpr int L_Q = L_OV + 128;
constexpr int L_FLAG = L_Q + 384;
constexpr int L_WO = L_FLAG + 4;
constexpr int L_END = L_WO + 1792 * 4;

__global__ void __launch_bounds__(NTHR, LB) k_fused(Params p) {
    __shared__ __attribute__((aligned(16))) float lds[L_END];
    LAS float* L = (LAS float*)lds;
    const int b = blockIdx.x, t = threadIdx.x, lane = t & 63, w = __builtin_amdgcn_readfirstlane(t >> 6);
    const int gA = b >> 5, iA = b & 31, hB = b >> 4, jB = b & 15;
    const int it = (b & 7) * 32 + (b >> 3);
    const rsrc_t rws = __builtin_amdgcn_make_buffer_rsrc((void*)p.ws, 0, WS_END, 0x00020000);
    STAMP_DECL;
    STAMP(0);
    {
        int off = -1; unsigned val = INVALID;
        if (t < 64) off = WS_PA + ((2 * gA) * 32 + iA) * 1024 + t * 16;
        else if (t < 128) off = WS_PA + ((2 * gA + 1) * 32 + iA) * 1024 + (t - 64) * 16;
        else if (t < 192) off = WS_PB + (hB * 16 + jB) * 1024 + (t - 128) * 16;
        else if (t < 220) off = WS_Y + ((((it >> 7) * 4 + (it & 3)) * 32 + ((it >> 2) & 31)) * 112) * 4 + (t - 192) * 16;
        else if (t < 222) off = WS_ML + ((2 * gA + (t - 220)) * 32 + iA) * 16;
        else if (t == 222) off = WS_MLB + (hB * 16 + jB) * 16;
        else if (t == 223 && b < 16) { off = WS_CTR + b * 256; val = 0u; }
        if (off >= 0) st128c(v4u{val, val, val, val}, rws, off);
    }
    SB();
    const int qpr = it >> 7, qcol0 = (it & 127) * 32;
    const int rg = t >> 2, ch = t & 3;
    v4u wv[7]; unsigned short xv[7];
    {
        const rsrc_t rwq = __builtin_amdgcn_make_buffer_rsrc((void*)p.wq[qpr], 0, DM * NQ * 2, 0x00020000);
        const rsrc_t rx = __builtin_amdgcn_make_buffer_rsrc((void*)p.x[qpr], 0, DM * 2, 0x00020000);
#pragma unroll
        for (int i = 0; i < 7; ++i) wv[i] = ldnt(rwq, rg * (NQ * 2) + qcol0 * 2 + ch * 16, i * (64 * NQ * 2));
#pragma unroll
        for (int i = 0; i < 7; ++i) xv[i] = __builtin_amdgcn_raw_buffer_load_b16(rx, rg * 2 + i * 128, 0, WQ_AUX);
    }
    v2u ep = {0u, 0u};
    if (w == 0) ep = ld64c(rws, WS_EPOCH);
    SB();
    const rsrc_t rkA = __builtin_amdgcn_make_buffer_rsrc((void*)p.k[0], 0, 8u * SK * DH * 2u, 0x00020000);
    const rsrc_t rvA = __builtin_amdgcn_make_buffer_rsrc((void*)p.v[0], 0, 8u * SK * DH * 2u, 0x00020000);
    const rsrc_t rkB = __builtin_amdgcn_make_buffer_rsrc((void*)p.k[1], 0, 16u * SK * DH * 2u, 0x00020000);
    const rsrc_t rvB = __builtin_amdgcn_make_buffer_rsrc((void*)p.v[1], 0, 16u * SK * DH * 2u, 0x00020000);
    const int vo = (lane >> 3) * 512 + (lane & 7) * 16;
#define PCX(AUX, r, o, j) __builtin_amdgcn_raw_buffer_load_b128(r, vo + ((j) & 3) * 128, (o) + ((j) >> 2) * 4096, AUX)
    constexpr unsigned strideA = PATA == 0 ? WAVES * TILE_B : 32 * WAVES * TILE_B, strideB = PATB == 0 ? WAVES * TILE_B : 16 * WAVES * TILE_B;
    const unsigned baseA = (unsigned)gA * (SK * 512u) + (unsigned)iA * (PATA == 0 ? NTA * WAVES * TILE_B : WAVES * TILE_B) + (unsigned)w * TILE_B;
    const unsigned headB = (unsigned)hB * (SK * 512u);
    const unsigned baseB = headB + (unsigned)jB * (PATB == 0 ? NSB * WAVES * TILE_B : WAVES * TILE_B) + (unsigned)w * TILE_B;
    v4u RK[8], RV[8];
    v4u RX[8];
    const int cpr = it >> 7, chq = (it >> 3) & 15, chf = (it >> 2) & 1, ccb = it & 3;
#pragma unroll
    for (int j = 0; j < 8; ++j) RK[j] = PCX(AUX_NT, rkA, baseA, j);
    SB();
    asm volatile("s_waitcnt vmcnt(8)" : "+v"(wv[0]), "+v"(wv[1]), "+v"(wv[2]), "+v"(wv[3]), "+v"(wv[4]), "+v"(wv[5]), "+v"(wv[6]),
                 "+v"(xv[0]), "+v"(xv[1]), "+v"(xv[2]), "+v"(xv[3]), "+v"(xv[4]), "+v"(xv[5]), "+v"(xv[6]), "+v"(ep)::"memory");
    STAMP(10);
    if (w != 0) {
#pragma unroll
        for (int j = 0; j < 8; ++j) RV[j] = PCX(AUX_NT, rvA, baseA, j);
    } else {
#pragma unroll
        for (int j = 0; j < 8; ++j) asm volatile("" : "=v"(RV[j]));
    }
    SB();
    {
        float acc[8];
#pragma unroll
        for (int e = 0; e < 8; ++e) acc[e] = 0.f;
#pragma unroll
        for (int i = 0; i < 7; ++i) {
            const float xf = asf((unsigned)xv[i] << 16);
#pragma unroll
            for (int e = 0; e < 4; ++e) { acc[2 * e] = fmaf(xf, bflo(wv[i][e]), acc[2 * e]); acc[2 * e + 1] = fmaf(xf, bfhi(wv[i][e]), acc[2 * e + 1]); }
        }
#pragma unroll
        for (int e = 0; e < 8; ++e) { float v = acc[e]; v += dpp<0x124>(v);   v += dpp<ROR8>(v); acc[e] = v; }
        float b4[4], c2[2];
#pragma unroll
        for (int e = 0; e < 4; ++e) b4[e] = swap16_add(acc[e], acc[e + 4]);
#pragma unroll
        for (int e = 0; e < 2; ++e) c2[e] = swap32_add(b4[e], b4[e + 2]);
        if ((lane & 12) == 0) *(LAS f32x2*)(L + L_RED + w * 32 + ch * 8 + 2 * (lane >> 5) + 4 * ((lane >> 4) & 1)) = f32x2{c2[0], c2[1]};
        if (t == 0) L[L_FLAG] = 0.f;
    }
    BARRIER();
    STAMP(11);
    u64 E = 0, tag = 0;
    if (w == 0) {
        E = ((u64)ep.y << 32) | ep.x;
        tag = (E + 1) & 0xffffffffffffull;
        if (lane < 32) {
            const float v = (L[L_RED + lane] + L[L_RED + 32 + lane]) + (L[L_RED + 64 + lane] + L[L_RED + 96 + lane]);
            const u64 g = (tag << 16) | (cvtpk(v, 0.f) & 0xffffu);
            st64c(v2u{(unsigned)g, (unsigned)(g >> 32)}, rws, WS_QG + (qpr * NQ + qcol0 + lane) * 8);
        } else if (lane == 32) {
            st64c(v2u{(unsigned)tag, (unsigned)(tag >> 32)}, rws, WS_READY + b * 8);
        }
        STAMP(1);
        const unsigned glo = (unsigned)tag << 16, ghi = (unsigned)(tag >> 16);
        v4u g[6];
        for (unsigned spins = 0;; ++spins) {
#pragma unroll
            for (int r = 0; r < 3; ++r) {
                const int row = r == 0 ? 2 * gA : r == 1 ? 2 * gA + 1 : 16 + hB;
                g[2 * r] = ld128c(rws, WS_QG + row * (DH * 8) + lane * 16);
                g[2 * r + 1] = ld128c(rws, WS_QG + row * (DH * 8) + 1024 + lane * 16);
            }
            unsigned bad = 0;
#pragma unroll
            for (int r = 0; r < 6; ++r) bad |= ((g[r].x ^ glo) >> 16) | (g[r].y ^ ghi) | ((g[r].z ^ glo) >> 16) | (g[r].w ^ ghi);
            const bool ok = bad == 0;
            if (all_lanes(ok) || spins > SPIN_CAP) break;
            __builtin_amdgcn_s_sleep(1);
        }
        LAS unsigned* qs = (LAS unsigned*)(L + L_Q);
#pragma unroll
        for (int r = 0; r < 6; ++r) qs[(r >> 1) * 128 + (r & 1) * 64 + lane] = (g[r].x & 0xffffu) | (g[r].z << 16);
        SB();
#pragma unroll
        for (int j = 0; j < 8; ++j) asm volatile("" ::"v"(RV[j]));
#pragma unroll
        for (int j = 0; j < 8; ++j) RV[j] = PCX(AUX_NT, rvA, baseA, j);
#pragma unroll
        for (int j = 0; j < 8; ++j) RX[j] = PCX(AUX_NT, rkA, baseA + strideA, j);
        SB();
    } else {
        const rsrc_t rwo = __builtin_amdgcn_make_buffer_rsrc((void*)p.wo[cpr], 0, DM * NQ * 2, 0x00020000);
        const int wob = ((chq * 256 + chf * 128) * DM + ccb * 112) * 2;
        v4u wo[10];
#pragma unroll
        for (int i = 0; i < 10; ++i) { const int e = t - 64 + 192 * i; const int ec = e < 1792 ? e : 0; wo[i] = ldnt(rwo, wob + (ec / 14) * (DM * 2) + (ec % 14) * 16, 0); }
#pragma unroll
        for (int i = 0; i < 10; ++i) { const int e = t - 64 + 192 * i; PIN(wo[i]); if (e < 1792) *(LAS v4u*)(L + L_WO + e * 4) = wo[i]; }
        SB();
#pragma unroll
        for (int j = 0; j < 8; ++j) RX[j] = PCX(AUX_NT, rkA, baseA + strideA, j);
        SB();
    }
    BARRIER();
    v4u qA[2][4];
    {
        const LAS unsigned char* qs = (const LAS unsigned char*)(L + L_Q);
#pragma unroll
        for (int jj = 0; jj < 4; ++jj) {
            const int d = 16 * ((lane & 7) + 8 * jj);
            qA[0][jj] = *(const LAS v4u*)(qs + d); qA[1][jj] = *(const LAS v4u*)(qs + 512 + d);
        }
    }
    GAS unsigned* ctr = (GAS unsigned*)(p.ws + WS_CTR) + hB * 64;
    asm volatile("" : "+v"(ctr));
    STAMP(2);
    {
        State<2> s; state_init(s);
        unsigned oc = baseA;
        int i = 0;
        {
            float x0[2] = {0.f, 0.f}, x1[2] = {0.f, 0.f};
#pragma unroll
            for (int j = 0; j < 8; ++j) {
                if (j < 4) { qk_piece<2>(RK[j], qA, j & 3, x0); PIN(x0[0]); PIN(x0[1]); } else { qk_piece<2>(RK[j], qA, j & 3, x1); PIN(x1[0]); PIN(x1[1]); }
                RK[j] = RX[j]; PIN(RK[j]);
                SB();
            }
            oc += strideA;
            float p0[2], p1[2];
            softmax_step<2>(x0, x1, s, p0, p1);
            SB();
#pragma unroll
            for (int j = 0; j < 8; ++j) {
                if (j < 4) pv_piece<2>(RV[j], p0, j & 3, s); else pv_piece<2>(RV[j], p1, j & 3, s);
                PIN4(s.acc[0], (j & 3) * 4); PIN4(s.acc[1], (j & 3) * 4);
                RV[j] = PCX(AUX_VA, rvA, oc, j);
                SB();
            }
            i = 1;
        }
#pragma unroll 1
        for (; i < NTA; ++i) {
            const bool last = (i == NTA - 1);
            const rsrc_t rkn = last ? rkB : rkA, rvn = last ? rvB : rvA;
            const unsigned on = last ? baseB : oc + strideA;
            float x0[2] = {0.f, 0.f}, x1[2] = {0.f, 0.f};
#pragma unroll
            for (int j = 0; j < 8; ++j) {
                if (j < 4) { qk_piece<2>(RK[j], qA, j & 3, x0); PIN(x0[0]); PIN(x0[1]); } else { qk_piece<2>(RK[j], qA, j & 3, x1); PIN(x1[0]); PIN(x1[1]); }
                RK[j] = PCX(AUX_KA, rkn, on, j);
                SB();
            }
            float p0[2], p1[2];
            softmax_step<2>(x0, x1, s, p0, p1);
            SB();
#pragma unroll
            for (int j = 0; j < 8; ++j) {
                if (j < 4) pv_piece<2>(RV[j], p0, j & 3, s); else pv_piece<2>(RV[j], p1, j & 3, s);
                PIN4(s.acc[0], (j & 3) * 4); PIN4(s.acc[1], (j & 3) * 4);
                RV[j] = PCX(AUX_VA, rvn, on, j);
                SB();
            }
            oc = on;
        }
        flush_state<2>(s, L + L_REC + (w * 3) * REC, lane);
    }
    STAMP(3);
    if (w == 0) {
        const unsigned tlo = (unsigned)tag, thi = (unsigned)(tag >> 32);
        for (unsigned spins = 0;; ++spins) {
            const v4u rd0 = ld128c(rws, WS_READY + lane * 32), rd1 = ld128c(rws, WS_READY + lane * 32 + 16);
            const bool ok = ((rd0.x ^ tlo) | (rd0.y ^ thi) | (rd0.z ^ tlo) | (rd0.w ^ thi) | (rd1.x ^ tlo) | (rd1.y ^ thi) | (rd1.z ^ tlo) | (rd1.w ^ thi)) == 0;
            if (all_lanes(ok) || spins > SPIN_CAP) break;
            __builtin_amdgcn_s_sleep(2);
        }
        *(volatile LAS unsigned*)(L + L_FLAG) = 1u;
        if (b == 0 && lane == 0) { const u64 e1 = E + 1; st64c(v2u{(unsigned)e1, (unsigned)(e1 >> 32)}, rws, WS_EPOCH); }
    }
    const int rgp = lane >> 4, cc = (lane & 15) < 14 ? (lane & 15) : 13;
    f32x2 wf[8][4];
#pragma unroll
    for (int k = 0; k < 8; ++k) {
        const v4u wr = *(const LAS v4u*)(L + L_WO + ((32 * w + 8 * rgp + k) * 14 + cc) * 4);
#pragma unroll
        for (int e = 0; e < 4; ++e) wf[k][e] = f32x2{bflo(wr[e]), bfhi(wr[e])};
    }
#pragma unroll
    for (int k = 0; k < 8; ++k) asm volatile("" : "+v"(wf[k][0]), "+v"(wf[k][1]), "+v"(wf[k][2]), "+v"(wf[k][3]));
    {
        v4u qB[1][4];
        {
            const LAS unsigned char* qs = (const LAS unsigned char*)(L + L_Q);
#pragma unroll
            for (int jj = 0; jj < 4; ++jj) qB[0][jj] = *(const LAS v4u*)(qs + 1024 + 16 * ((lane & 7) + 8 * jj));
        }
        State<1> s; state_init(s);
        const rsrc_t rnull = __builtin_amdgcn_make_buffer_rsrc((void*)p.k[1], 0, 0, 0x00020000);
        unsigned on = baseB + strideB;
        int n = 1;
        bool have = true;
        bool onhalf = false, curhalf = false;
        bool more;
        do {
            unsigned tk = 0xffffffffu;
            if (NPOOL > 0 && n + 1 >= NSB && have) {
                if (n + 1 == NSB && w != 0) { for (unsigned spins = 0; *(volatile LAS unsigned*)(L + L_FLAG) == 0u && spins < (SPIN_CAP << 4); ++spins) __builtin_amdgcn_s_sleep(1); }
                if (lane == 0) tk = __hip_atomic_fetch_add(ctr, 1u, __ATOMIC_RELAXED, __HIP_MEMORY_SCOPE_AGENT);
            }
            const rsrc_t rk = have ? rkB : rnull, rv = have ? rvB : rnull;
            const rsrc_t rk2 = (have && !onhalf) ? rkB : rnull, rv2 = (have && !onhalf) ? rvB : rnull;
            SB();
            float x0[1] = {0.f}, x1[1] = {0.f};
            float p0[1], p1[1];
#pragma unroll
            for (int j = 0; j < 8; ++j) {
                if (j < 4) { qk_piece<1>(RK[j], qB, j & 3, x0); PIN(x0[0]); } else { qk_piece<1>(RK[j], qB, j & 3, x1); PIN(x1[0]); }
                RK[j] = PCX(AUX_KB, j < 4 ? rk : rk2, on, j);
                SB();
            }
            softmax_step<1>(x0, x1, s, p0, p1, curhalf);
            SB();
#pragma unroll
            for (int j = 0; j < 8; ++j) {
                if (j < 4) pv_piece<1>(RV[j], p0, j & 3, s); else pv_piece<1>(RV[j], p1, j & 3, s);
                PIN4(s.acc[0], (j & 3) * 4);
                RV[j] = PCX(AUX_VB, j < 4 ? rv : rv2, on, j);
                SB();
            }
            more = have;
            curhalf = onhalf;
            ++n;
            if (n < NSB) on += strideB;
            else {
                const unsigned tt = __builtin_amdgcn_readfirstlane(tk);
                have = tt < (unsigned)NTICK; onhalf = tt >= (unsigned)NFULL;
                on = headB + POOL0 * TILE_B + (onhalf ? NFULL * TILE_B + (tt - NFULL) * (TILE_B / 2) : tt * TILE_B);
            }
        } while (more);
        flush_state<1>(s, L + L_REC + (w * 3 + 2) * REC, lane);
    }
    STAMP(8);
    BARRIER();
    STAMP(4);
    if (t < 192) {
        const int hs = w, d0 = lane * 4;
        float M = -1e30f;
#pragma unroll
        for (int ww = 0; ww < WAVES; ++ww) M = fmaxf(M, L[L_REC + (ww * 3 + hs) * REC + 256]);
        f32x4 a = {0.f, 0.f, 0.f, 0.f}; float Ls = 0.f;
#pragma unroll
        for (int ww = 0; ww < WAVES; ++ww) {
            const LAS float* rec = L + L_REC + (ww * 3 + hs) * REC;
            const float wt = ex2(rec[256] - M);
            a += *(const LAS f32x4*)(rec + d0) * wt; Ls = fmaf(rec[257], wt, Ls);
        }
        int dst, ml;
        if (hs < 2) { const int hq = 2 * gA + hs; dst = WS_PA + (hq * 32 + iA) * 1024; ml = WS_ML + (hq * 32 + iA) * 16; }
        else { dst = WS_PB + (hB * 16 + jB) * 1024; ml = WS_MLB + (hB * 16 + jB) * 16; }
        st128c(__builtin_bit_cast(v4u, a), rws, dst + lane * 16);
        if (lane == 0) st64c(v2u{asu(M), asu(Ls)}, rws, ml);
    }
    {
        const int S8 = cpr == 0 ? 4 : 2;
        const int sg = lane >> 3, cl = lane & 7;
        const int mlb = (cpr == 0 ? WS_ML : WS_MLB) + (chq * S8 * 8 + sg) * 16;
        const int pb = (cpr == 0 ? WS_PA : WS_PB) + (chq * S8 * 8 + sg) * 1024 + chf * 512 + (8 * w + cl) * 16;
        v2u ml[4]; v4u d[4];
        for (unsigned spins = 0;; ++spins) {
#pragma unroll
            for (int i = 0; i < 4; ++i) ml[i] = ld64c(rws, mlb + (i < S8 ? i : 0) * 128);
            unsigned mx = 0;
#pragma unroll
            for (int i = 0; i < 4; ++i) mx = max(mx, max(ml[i].x, ml[i].y));
            if (all_lanes(mx != INVALID) || spins > SPIN_CAP) break;
            __builtin_amdgcn_s_sleep(POLL_SLEEP);
        }
        for (unsigned spins = 0;; ++spins) {
#pragma unroll
            for (int i = 0; i < 4; ++i) d[i] = ld128c(rws, pb + (i < S8 ? i : 0) * 8192);
            unsigned mx = 0;
#pragma unroll
            for (int i = 0; i < 4; ++i) mx = max(mx, umax4(d[i]));
            if (all_lanes(mx != INVALID) || spins > SPIN_CAP) break;
            __builtin_amdgcn_s_sleep(1);
        }
        STAMP(5);
        float M = -1e30f;
#pragma unroll
        for (int i = 0; i < 4; ++i) if (i < S8) M = fmaxf(M, asf(ml[i].x));
        M = xmax_groups(M);
        float Lt = 0.f; f32x4 a = {0.f, 0.f, 0.f, 0.f};
#pragma unroll
        for (int i = 0; i < 4; ++i) if (i < S8) { const float wt = ex2(asf(ml[i].x) - M); Lt = fmaf(wt, asf(ml[i].y), Lt); a += __builtin_bit_cast(f32x4, d[i]) * wt; }
        Lt = xsum_groups(Lt);
        const float inv = 1.0f / Lt;
        f32x4 o4;
#pragma unroll
        for (int e = 0; e < 4; ++e) o4[e] = bflo(cvtpk(xsum_groups(a[e]) * inv, 0.f));
        if (lane < 8) *(LAS f32x4*)(L + L_OV + 32 * w + 4 * lane) = o4;
        asm volatile("s_waitcnt lgkmcnt(0)" ::: "memory");
        const f32x4 oa = *(const LAS f32x4*)(L + L_OV + 32 * w + 8 * rgp), ob = *(const LAS f32x4*)(L + L_OV + 32 * w + 8 * rgp + 4);
        f32x2 y2[4] = {f32x2{0.f, 0.f}, f32x2{0.f, 0.f}, f32x2{0.f, 0.f}, f32x2{0.f, 0.f}};
#pragma unroll
        for (int k = 0; k < 8; ++k) {
            const float o = k < 4 ? oa[k & 3] : ob[k & 3];
#pragma unroll
            for (int e = 0; e < 4; ++e) y2[e] = __builtin_elementwise_fma(f32x2{o, o}, wf[k][e], y2[e]);
        }
        const float y[8] = {y2[0][0], y2[0][1], y2[1][0], y2[1][1], y2[2][0], y2[2][1], y2[3][0], y2[3][1]};
        float b4[4], c2[2];
#pragma unroll
        for (int e = 0; e < 4; ++e) b4[e] = swap16_add(y[e], y[e + 4]);
#pragma unroll
        for (int e = 0; e < 2; ++e) c2[e] = swap32_add(b4[e], b4[e + 2]);
        if ((lane & 15) < 14) *(LAS f32x2*)(L + L_RED + w * 128 + cc * 8 + 2 * (lane >> 5) + 4 * ((lane >> 4) & 1)) = f32x2{c2[0], c2[1]};
    }
    BARRIER();
    if (t < 28) {
        const f32x4 v = (*(const LAS f32x4*)(L + L_RED + t * 4) + *(const LAS f32x4*)(L + L_RED + 128 + t * 4)) +
                        (*(const LAS f32x4*)(L + L_RED + 256 + t * 4) + *(const LAS f32x4*)(L + L_RED + 384 + t * 4));
        st128c(__builtin_bit_cast(v4u, v), rws, WS_Y + (((cpr * 4 + ccb) * 32 + chq * 2 + chf) * 112) * 4 + t * 16);
    }
    STAMP(6);
    if ((it & 7) == 0 && t < 224) {
        const int r = it >> 3, fpr = r >> 4, fcb = (r >> 2) & 3, sub = r & 3;
        const int c = t >> 5, s = t & 31;
        const int off = WS_Y + (((fpr * 4 + fcb) * 32 + s) * 112 + sub * 28 + c * 4) * 4;
        v4u d;
        for (unsigned spins = 0;; ++spins) {
            d = ld128c(rws, off);
            if (all_lanes(valid4(d)) || spins > SPIN_CAP) break;
            __builtin_amdgcn_s_sleep(1);
        }
        float v[4] = {asf(d.x), asf(d.y), asf(d.z), asf(d.w)};
#pragma unroll
        for (int e = 0; e < 4; ++e) { float z = v[e]; z += dpp<XOR1>(z); z += dpp<XOR2>(z); z += dpp<HMIRROR>(z); z += dpp<ROR8>(z); v[e] = x16_sum(z); }
        if (s == 0) *(GAS v2u*)(p.out + fpr * DM + fcb * 112 + sub * 28 + c * 4) = v2u{cvtpk(v[0], v[1]), cvtpk(v[2], v[3])};
    }
    STAMP(7);
    STAMP_FLUSH();
}
}

extern "C" void kernel_launch(void* const* d_in, const int* in_sizes, int n_in, void* d_out, int out_size, void* d_ws, size_t ws_size,
                              hipStream_t stream) {
    Params p;
    p.x[0] = (const unsigned short*)d_in[0]; p.k[0] = (const unsigned char*)d_in[1]; p.v[0] = (const unsigned char*)d_in[2];
    p.wq[0] = (const unsigned short*)d_in[3]; p.wo[0] = (const unsigned short*)d_in[4];
    p.x[1] = (const unsigned short*)d_in[5]; p.k[1] = (const unsigned char*)d_in[6]; p.v[1] = (const unsigned char*)d_in[7];
    p.wq[1] = (const unsigned short*)d_in[8]; p.wo[1] = (const unsigned short*)d_in[9];
    p.out = (unsigned short*)d_out; p.ws = (unsigned char*)d_ws;
    k_fused<<<NWG, NTHR, 0, stream>>>(p);
}
```

```cpp
#include <hip/hip_runtime.h>
#include <hip/hip_bf16.h>
#include <stdint.h>

#define GAS __attribute__((address_space(1)))
#define LAS __attribute__((address_space(3)))
typedef unsigned v4u __attribute__((ext_vector_type(4)));
typedef unsigned v2u __attribute__((ext_vector_type(2)));
typedef float f32x2 __attribute__((ext_vector_type(2)));
typedef float f32x4 __attribute__((ext_vector_type(4)));
typedef __bf16 bf16x2 __attribute__((ext_vector_type(2)));
typedef __amdgpu_buffer_rsrc_t rsrc_t;
typedef unsigned long long u64;

namespace {
constexpr int NWG = 256, WAVES = 4, NTHR = WAVES * 64;
constexpr int SK = 32768, DH = 256, DM = 448, NQ = 4096;
constexpr int TK = 16;
constexpr int TILE_B = TK * DH * 2;
constexpr int NTA = (SK / 32) / TK / WAVES;
constexpr int NTB_ALL = (SK / 16) / TK / WAVES;
constexpr int NSB = 18;
constexpr int NHALF = 256;
constexpr int POOL0 = NSB * 16 * WAVES;
constexpr int NPOOL = (NTB_ALL - NSB) * 16 * WAVES;
constexpr int NFULL = NPOOL - NHALF / 2;
constexpr int NTICK = NFULL + NHALF;
static_assert(NSB >= 2 && NSB < NTB_ALL && NHALF % 2 == 0 && NFULL >= 0, "pool shape");
constexpr float SC = 0.0625f * 1.4426950408889634f;
constexpr float M0 = -1e30f;
constexpr unsigned INVALID = 0xffffffffu;
constexpr unsigned SPIN_CAP = 1u << 18;

constexpr unsigned WS_EPOCH = 0;
constexpr unsigned WS_READY = 4096;
constexpr unsigned WS_CTR = 8192;
constexpr unsigned WS_QG = 16384;
constexpr unsigned WS_MLA = WS_QG + 65536;
constexpr unsigned WS_MLB = WS_MLA + 16 * 32 * 16;
constexpr unsigned WS_PA = 131072;
constexpr unsigned WS_PB = WS_PA + 16 * 32 * 1024;
constexpr unsigned WS_Y = WS_PB + 16 * 16 * 1024;
constexpr unsigned WS_END = WS_Y + 2 * 4 * 32 * 112 * 4;

struct Params {
    const unsigned short* x[2];
    const unsigned char* k[2];
    const unsigned char* v[2];
    const unsigned short* wq[2];
    const unsigned short* wo[2];
    unsigned short* out;
    unsigned char* ws;
};

__device__ __forceinline__ float asf(unsigned u) { return __builtin_bit_cast(float, u); }
__device__ __forceinline__ unsigned asu(float f) { return __builtin_bit_cast(unsigned, f); }
__device__ __forceinline__ float dot2(unsigned a, unsigned b, float c) {
    return __builtin_amdgcn_fdot2_f32_bf16(__builtin_bit_cast(bf16x2, a), __builtin_bit_cast(bf16x2, b), c, false);
}
__device__ __forceinline__ unsigned cvtpk(float lo, float hi) {
    bf16x2 r = {(__bf16)lo, (__bf16)hi};
    return __builtin_bit_cast(unsigned, r);
}
template <int CTRL> __device__ __forceinline__ float dpp(float x) { return asf((unsigned)__builtin_amdgcn_mov_dpp((int)asu(x), CTRL, 0xf, 0xf, true)); }
constexpr int XOR1 = 0xB1, XOR2 = 0x4E, HMIRROR = 0x141, ROR4 = 0x124, ROR8 = 0x128;
__device__ __forceinline__ float bflo(unsigned u) { return asf(u << 16); }
__device__ __forceinline__ float bfhi(unsigned u) { return asf(u & 0xffff0000u); }
__device__ __forceinline__ float ex2(float x) { return __builtin_amdgcn_exp2f(x); }
constexpr int AUX_NT = 2, AUX_SC1 = 16, RSRC_W3 = 0x00020000;
__device__ __forceinline__ rsrc_t rsrc(const void* p, unsigned bytes) { return __builtin_amdgcn_make_buffer_rsrc((void*)p, 0, bytes, RSRC_W3); }
__device__ __forceinline__ v4u ldnt(rsrc_t r, int voff, unsigned soff) { return __builtin_amdgcn_raw_buffer_load_b128(r, voff, soff, AUX_NT); }
__device__ __forceinline__ v4u ld128c(rsrc_t r, int voff) { return __builtin_amdgcn_raw_buffer_load_b128(r, voff, 0, AUX_SC1); }
__device__ __forceinline__ v2u ld64c(rsrc_t r, int voff) { return __builtin_amdgcn_raw_buffer_load_b64(r, voff, 0, AUX_SC1); }
__device__ __forceinline__ void st128c(v4u v, rsrc_t r, int voff) { __builtin_amdgcn_raw_buffer_store_b128(v, r, voff, 0, AUX_SC1); }
__device__ __forceinline__ void st64c(v2u v, rsrc_t r, int voff) { __builtin_amdgcn_raw_buffer_store_b64(v, r, voff, 0, AUX_SC1); }
__device__ __forceinline__ void st64c(u64 v, rsrc_t r, int voff) { st64c(v2u{(unsigned)v, (unsigned)(v >> 32)}, r, voff); }
__device__ __forceinline__ float swap32_add(float a, float b) { auto r = __builtin_amdgcn_permlane32_swap(asu(a), asu(b), false, false); return asf(r[0]) + asf(r[1]); }
__device__ __forceinline__ float swap16_add(float a, float b) { auto r = __builtin_amdgcn_permlane16_swap(asu(a), asu(b), false, false); return asf(r[0]) + asf(r[1]); }
__device__ __forceinline__ float x16_max(float x) { auto s = __builtin_amdgcn_permlane16_swap(asu(x), asu(x), false, false); return fmaxf(asf(s[0]), asf(s[1])); }
__device__ __forceinline__ float x32_max(float x) { auto s = __builtin_amdgcn_permlane32_swap(asu(x), asu(x), false, false); return fmaxf(asf(s[0]), asf(s[1])); }
__device__ __forceinline__ float xmax_groups(float x) { x = fmaxf(x, dpp<ROR8>(x)); return x32_max(x16_max(x)); }
__device__ __forceinline__ float xsum_groups(float x) { x = x + dpp<ROR8>(x); x = swap16_add(x, x); return swap32_add(x, x); }
__device__ __forceinline__ bool all_lanes(bool ok) { return __builtin_amdgcn_ballot_w64(ok) == __builtin_amdgcn_ballot_w64(true); }
__device__ __forceinline__ unsigned umax4(v4u v) { return max(max(v.x, v.y), max(v.z, v.w)); }
__device__ __forceinline__ unsigned tagdiff(v4u g, unsigned lo, unsigned hi) { return ((g.x ^ lo) >> 16) | (g.y ^ hi) | ((g.z ^ lo) >> 16) | (g.w ^ hi); }
#define PIN(x) asm volatile("" : "+v"(x)::"memory")
#define PIN4(a, o) asm volatile("" : "+v"((a)[(o)]), "+v"((a)[(o) + 1]), "+v"((a)[(o) + 2]), "+v"((a)[(o) + 3])::"memory")
#define SB() __builtin_amdgcn_sched_barrier(0)
#define BARRIER() do { asm volatile("s_waitcnt lgkmcnt(0)" ::: "memory"); __builtin_amdgcn_s_barrier(); asm volatile("" ::: "memory"); } while (0)
#define DRAIN() asm volatile("s_waitcnt vmcnt(0)" ::: "memory")

template <int G> struct State { float m[G], l[G]; f32x2 acc[G][16]; };

template <int G>
__device__ __forceinline__ void state_init(State<G>& s) {
#pragma unroll
    for (int g = 0; g < G; ++g) {
        s.m[g] = M0; s.l[g] = 0.f;
#pragma unroll
        for (int i = 0; i < 16; ++i) s.acc[g][i] = f32x2{0.f, 0.f};
    }
}
template <int G>
__device__ __forceinline__ void qk_piece(const v4u& kv, const v4u (&q)[G][4], int jj, float (&x)[G]) {
#pragma unroll
    for (int g = 0; g < G; ++g) {
        float a = x[g];
#pragma unroll
        for (int i = 0; i < 4; ++i) a = dot2(kv[i], q[g][jj][i], a);
        x[g] = a;
    }
}
template <int G>
__device__ __forceinline__ void softmax_step(float (&x0)[G], float (&x1)[G], State<G>& s, float (&p0)[G], float (&p1)[G], bool half) {
#pragma unroll
    for (int g = 0; g < G; ++g) {
        float a = x0[g], b = x1[g];
        a += dpp<XOR1>(a); b += dpp<XOR1>(b);
        a += dpp<XOR2>(a); b += dpp<XOR2>(b);
        a += dpp<HMIRROR>(a); b += dpp<HMIRROR>(b);
        const unsigned pk = cvtpk(a, b);
        const float sa = bflo(pk) * SC, sb = half ? M0 : bfhi(pk) * SC;
        const float mn = fmaxf(s.m[g], fmaxf(sa, sb));
        const float al = ex2(s.m[g] - mn);
        p0[g] = ex2(sa - mn); p1[g] = ex2(sb - mn);
        s.l[g] = fmaf(s.l[g], al, p0[g] + p1[g]);
        s.m[g] = mn;
#pragma unroll
        for (int i = 0; i < 16; ++i) s.acc[g][i] *= al;
    }
}
template <int G>
__device__ __forceinline__ void pv_piece(const v4u& vv, const float (&p)[G], int jj, State<G>& s) {
#pragma unroll
    for (int i = 0; i < 4; ++i) {
        const f32x2 vf = {bflo(vv[i]), bfhi(vv[i])};
#pragma unroll
        for (int g = 0; g < G; ++g) s.acc[g][jj * 4 + i] = __builtin_elementwise_fma(vf, f32x2{p[g], p[g]}, s.acc[g][jj * 4 + i]);
    }
}
constexpr int REC = 260;
template <int G>
__device__ __forceinline__ void flush_state(State<G>& s, LAS float* rec0, int lane) {
#pragma unroll
    for (int g = 0; g < G; ++g) {
        const float M = xmax_groups(s.m[g]);
        const float wt = ex2(s.m[g] - M);
        const float L = xsum_groups(s.l[g] * wt);
        float v[32], a[16], b8[8], c4[4];
#pragma unroll
        for (int i = 0; i < 16; ++i) { v[2 * i] = s.acc[g][i][0] * wt; v[2 * i + 1] = s.acc[g][i][1] * wt; }
#pragma unroll
        for (int i = 0; i < 16; ++i) a[i] = swap32_add(v[i], v[i + 16]);
#pragma unroll
        for (int i = 0; i < 8; ++i) b8[i] = swap16_add(a[i], a[i + 8]);
        const bool hi8 = (lane & 8) != 0;
#pragma unroll
        for (int i = 0; i < 4; ++i) { const float keep = hi8 ? b8[i + 4] : b8[i], send = hi8 ? b8[i] : b8[i + 4]; c4[i] = keep + dpp<ROR8>(send); }
        LAS float* rec = rec0 + g * REC;
        *(LAS f32x4*)(rec + 64 * (lane >> 4) + 8 * (lane & 7) + 4 * ((lane >> 3) & 1)) = f32x4{c4[0], c4[1], c4[2], c4[3]};
        if (lane == 0) { rec[256] = M; rec[257] = L; }
    }
}

constexpr int L_REC = 0;
constexpr int L_RED = WAVES * 3 * REC;
constexpr int L_OV = L_RED + WAVES * 128;
constexpr int L_Q = L_OV + 128;
constexpr int L_FLAG = L_Q + 384;
constexpr int L_WO = L_FLAG + 4;
constexpr int L_END = L_WO + 1792 * 4;

__global__ void __launch_bounds__(NTHR, 2) k_fused(Params p) {
    __shared__ __attribute__((aligned(16))) float lds[L_END];
    LAS float* L = (LAS float*)lds;
    const int b = blockIdx.x, t = threadIdx.x, lane = t & 63, w = __builtin_amdgcn_readfirstlane(t >> 6);
    const int gA = b >> 5, iA = b & 31;
    const int hB = b >> 4, jB = b & 15;
    const int it = (b & 7) * 32 + (b >> 3);

    const int qpr = it >> 7, qcol0 = (it & 127) * 32;
    const int rg = t >> 2, ch = t & 3;
    v4u wv[7]; unsigned short xv[7];
    {
        const rsrc_t rwq = rsrc(p.wq[qpr], DM * NQ * 2), rx = rsrc(p.x[qpr], DM * 2);
#pragma unroll
        for (int i = 0; i < 7; ++i) wv[i] = ldnt(rwq, rg * (NQ * 2) + qcol0 * 2 + ch * 16, i * (64 * NQ * 2));
#pragma unroll
        for (int i = 0; i < 7; ++i) xv[i] = __builtin_amdgcn_raw_buffer_load_b16(rx, rg * 2 + i * 128, 0, AUX_NT);
    }
    const rsrc_t rws = rsrc(p.ws, WS_END);
    v2u ep = {0u, 0u};
    if (w == 0) ep = ld64c(rws, WS_EPOCH);
    SB();
    const int cpr = it >> 7, chq = (it >> 3) & 15, chf = (it >> 2) & 1, ccb = it & 3;
    const int ybox = WS_Y + (((cpr * 4 + ccb) * 32 + chq * 2 + chf) * 112) * 4;
    int coff; unsigned cval = INVALID;
    if (w < 2) coff = WS_PA + ((2 * gA + w) * 32 + iA) * 1024 + lane * 16;
    else if (w == 2) coff = WS_PB + (hB * 16 + jB) * 1024 + lane * 16;
    else {
        coff = lane < 28 ? ybox + lane * 16 : lane < 30 ? WS_MLA + ((2 * gA + (lane - 28)) * 32 + iA) * 16 : lane == 30 ? WS_MLB + (hB * 16 + jB) * 16
             : (lane == 31 && b < 16) ? WS_CTR + b * 256 : (int)WS_END;
        if (lane == 31) cval = 0u;
    }
    PIN(coff); PIN(cval);
    SB();
    const rsrc_t rkA = rsrc(p.k[0], 8u * SK * DH * 2u), rvA = rsrc(p.v[0], 8u * SK * DH * 2u);
    const rsrc_t rkB = rsrc(p.k[1], 16u * SK * DH * 2u), rvB = rsrc(p.v[1], 16u * SK * DH * 2u);
    const int vo = (lane >> 3) * 512 + (lane & 7) * 16;
#define TILE_LD(r, o, j) __builtin_amdgcn_raw_buffer_load_b128(r, vo + ((j) & 3) * 128, (o) + ((j) >> 2) * 4096, AUX_NT)
    constexpr unsigned STRIDE = WAVES * TILE_B;
    const unsigned baseA = (unsigned)gA * (SK * 512u) + (unsigned)iA * (NTA * STRIDE) + (unsigned)w * TILE_B;
    const unsigned headB = (unsigned)hB * (SK * 512u);
    const unsigned baseB = headB + (unsigned)jB * (NSB * STRIDE) + (unsigned)w * TILE_B;
    v4u RK[8], RV[8], RX[8];
#pragma unroll
    for (int j = 0; j < 8; ++j) RK[j] = TILE_LD(rkA, baseA, j);
    SB();
    asm volatile("s_waitcnt vmcnt(8)" : "+v"(wv[0]), "+v"(wv[1]), "+v"(wv[2]), "+v"(wv[3]), "+v"(wv[4]), "+v"(wv[5]), "+v"(wv[6]),
                 "+v"(xv[0]), "+v"(xv[1]), "+v"(xv[2]), "+v"(xv[3]), "+v"(xv[4]), "+v"(xv[5]), "+v"(xv[6]), "+v"(ep)::"memory");
    st128c(v4u{cval, cval, cval, cval}, rws, coff);
    if (w != 0) {
#pragma unroll
        for (int j = 0; j < 8; ++j) RV[j] = TILE_LD(rvA, baseA, j);
    } else {
#pragma unroll
        for (int j = 0; j < 8; ++j) asm volatile("" : "=v"(RV[j]));
    }
    SB();
    {
        float acc[8];
#pragma unroll
        for (int e = 0; e < 8; ++e) acc[e] = 0.f;
#pragma unroll
        for (int i = 0; i < 7; ++i) {
            const float xf = asf((unsigned)xv[i] << 16);
#pragma unroll
            for (int e = 0; e < 4; ++e) { acc[2 * e] = fmaf(xf, bflo(wv[i][e]), acc[2 * e]); acc[2 * e + 1] = fmaf(xf, bfhi(wv[i][e]), acc[2 * e + 1]); }
        }
#pragma unroll
        for (int e = 0; e < 8; ++e) { float v = acc[e]; v += dpp<ROR4>(v); v += dpp<ROR8>(v); acc[e] = v; }
        float b4[4], c2[2];
#pragma unroll
        for (int e = 0; e < 4; ++e) b4[e] = swap16_add(acc[e], acc[e + 4]);
#pragma unroll
        for (int e = 0; e < 2; ++e) c2[e] = swap32_add(b4[e], b4[e + 2]);
        if ((lane & 12) == 0) *(LAS f32x2*)(L + L_RED + w * 32 + ch * 8 + 2 * (lane >> 5) + 4 * ((lane >> 4) & 1)) = f32x2{c2[0], c2[1]};
        if (t == 0) { L[L_FLAG] = 0.f; L[L_FLAG + 1] = 0.f; }
    }
    BARRIER();
    u64 epoch = 0, tag = 0;
    if (w == 0) {
        epoch = ((u64)ep.y << 32) | ep.x;
        tag = (epoch + 1) & 0xffffffffffffull;
        if (lane < 32) {
            const float v = (L[L_RED + lane] + L[L_RED + 32 + lane]) + (L[L_RED + 64 + lane] + L[L_RED + 96 + lane]);
            st64c((tag << 16) | (cvtpk(v, 0.f) & 0xffffu), rws, WS_QG + (qpr * NQ + qcol0 + lane) * 8);
        }
        const unsigned glo = (unsigned)tag << 16, ghi = (unsigned)(tag >> 16);
        v4u g[4];
        for (unsigned spins = 0;; ++spins) {
            unsigned bad = 0;
#pragma unroll
            for (int r = 0; r < 4; ++r) g[r] = ld128c(rws, WS_QG + (2 * gA + (r >> 1)) * (DH * 8) + (r & 1) * 1024 + lane * 16);
#pragma unroll
            for (int r = 0; r < 4; ++r) bad |= tagdiff(g[r], glo, ghi);
            if (all_lanes(bad == 0) || spins > SPIN_CAP) break;
            __builtin_amdgcn_s_sleep(1);
        }
        LAS unsigned* qs = (LAS unsigned*)(L + L_Q);
#pragma unroll
        for (int r = 0; r < 4; ++r) qs[(r >> 1) * 128 + (r & 1) * 64 + lane] = (g[r].x & 0xffffu) | (g[r].z << 16);
        DRAIN();
        SB();
#pragma unroll
        for (int j = 0; j < 8; ++j) asm volatile("" ::"v"(RV[j]));
#pragma unroll
        for (int j = 0; j < 8; ++j) RV[j] = TILE_LD(rvA, baseA, j);
#pragma unroll
        for (int j = 0; j < 8; ++j) RX[j] = TILE_LD(rkA, baseA + STRIDE, j);
        SB();
    } else {
        const rsrc_t rwo = rsrc(p.wo[cpr], DM * NQ * 2);
        const int wob = ((chq * 256 + chf * 128) * DM + ccb * 112) * 2;
        v4u wo[10];
#pragma unroll
        for (int i = 0; i < 10; ++i) { const int e = t - 64 + 192 * i; const int ec = e < 1792 ? e : 0; wo[i] = ldnt(rwo, wob + (ec / 14) * (DM * 2) + (ec % 14) * 16, 0); }
#pragma unroll
        for (int i = 0; i < 10; ++i) { const int e = t - 64 + 192 * i; PIN(wo[i]); if (e < 1792) *(LAS v4u*)(L + L_WO + e * 4) = wo[i]; }
        DRAIN();
        SB();
#pragma unroll
        for (int j = 0; j < 8; ++j) RX[j] = TILE_LD(rkA, baseA + STRIDE, j);
        SB();
    }
    BARRIER();
    v4u qA[2][4];
    {
        const LAS unsigned char* qs = (const LAS unsigned char*)(L + L_Q);
#pragma unroll
        for (int jj = 0; jj < 4; ++jj) {
            const int d = 16 * ((lane & 7) + 8 * jj);
            qA[0][jj] = *(const LAS v4u*)(qs + d); qA[1][jj] = *(const LAS v4u*)(qs + 512 + d);
        }
    }
    if (w == 0) {
        if (lane == 0) st64c(tag, rws, WS_READY + b * 8);
        const unsigned glo = (unsigned)tag << 16, ghi = (unsigned)(tag >> 16);
        v4u g0, g1;
        for (unsigned spins = 0;; ++spins) {
            g0 = ld128c(rws, WS_QG + (16 + hB) * (DH * 8) + lane * 16);
            g1 = ld128c(rws, WS_QG + (16 + hB) * (DH * 8) + 1024 + lane * 16);
            if (all_lanes((tagdiff(g0, glo, ghi) | tagdiff(g1, glo, ghi)) == 0) || spins > SPIN_CAP) break;
            __builtin_amdgcn_s_sleep(1);
        }
        LAS unsigned* qs = (LAS unsigned*)(L + L_Q);
        qs[256 + lane] = (g0.x & 0xffffu) | (g0.z << 16);
        qs[320 + lane] = (g1.x & 0xffffu) | (g1.z << 16);
        asm volatile("s_waitcnt lgkmcnt(0)" ::: "memory");
        *(volatile LAS unsigned*)(L + L_FLAG + 1) = 1u;
    }

    {
        State<2> s; state_init(s);
        unsigned oc = baseA + STRIDE;
        {
            float x0[2] = {0.f, 0.f}, x1[2] = {0.f, 0.f}, p0[2], p1[2];
#pragma unroll
            for (int j = 0; j < 8; ++j) {
                if (j < 4) { qk_piece<2>(RK[j], qA, j & 3, x0); PIN(x0[0]); PIN(x0[1]); } else { qk_piece<2>(RK[j], qA, j & 3, x1); PIN(x1[0]); PIN(x1[1]); }
                RK[j] = RX[j]; PIN(RK[j]);
                SB();
            }
            softmax_step<2>(x0, x1, s, p0, p1, false);
            SB();
#pragma unroll
            for (int j = 0; j < 8; ++j) {
                if (j < 4) pv_piece<2>(RV[j], p0, j & 3, s); else pv_piece<2>(RV[j], p1, j & 3, s);
                PIN4(s.acc[0], (j & 3) * 4); PIN4(s.acc[1], (j & 3) * 4);
                RV[j] = TILE_LD(rvA, oc, j);
                SB();
            }
        }
#pragma unroll 1
        for (int i = 1; i < NTA; ++i) {
            const bool last = (i == NTA - 1);
            const rsrc_t rkn = last ? rkB : rkA, rvn = last ? rvB : rvA;
            const unsigned on = last ? baseB : oc + STRIDE;
            float x0[2] = {0.f, 0.f}, x1[2] = {0.f, 0.f}, p0[2], p1[2];
#pragma unroll
            for (int j = 0; j < 8; ++j) {
                if (j < 4) { qk_piece<2>(RK[j], qA, j & 3, x0); PIN(x0[0]); PIN(x0[1]); } else { qk_piece<2>(RK[j], qA, j & 3, x1); PIN(x1[0]); PIN(x1[1]); }
                RK[j] = TILE_LD(rkn, on, j);
                SB();
            }
            softmax_step<2>(x0, x1, s, p0, p1, false);
            SB();
#pragma unroll
            for (int j = 0; j < 8; ++j) {
                if (j < 4) pv_piece<2>(RV[j], p0, j & 3, s); else pv_piece<2>(RV[j], p1, j & 3, s);
                PIN4(s.acc[0], (j & 3) * 4); PIN4(s.acc[1], (j & 3) * 4);
                RV[j] = TILE_LD(rvn, on, j);
                SB();
            }
            oc = on;
        }
        flush_state<2>(s, L + L_REC + (w * 3) * REC, lane);
    }
    if (w == 0) {
        const unsigned tlo = (unsigned)tag, thi = (unsigned)(tag >> 32);
        for (unsigned spins = 0;; ++spins) {
            const v4u r0 = ld128c(rws, WS_READY + lane * 32), r1 = ld128c(rws, WS_READY + lane * 32 + 16);
            const unsigned bad = (r0.x ^ tlo) | (r0.y ^ thi) | (r0.z ^ tlo) | (r0.w ^ thi) | (r1.x ^ tlo) | (r1.y ^ thi) | (r1.z ^ tlo) | (r1.w ^ thi);
            if (all_lanes(bad == 0) || spins > SPIN_CAP) break;
            __builtin_amdgcn_s_sleep(2);
        }
        *(volatile LAS unsigned*)(L + L_FLAG) = 1u;
        if (b == 0 && lane == 0) st64c(epoch + 1, rws, WS_EPOCH);
    }
    const int rgp = lane >> 4, cc = (lane & 15) < 14 ? (lane & 15) : 13;
    f32x2 wf[8][4];
#pragma unroll
    for (int k = 0; k < 8; ++k) {
        const v4u wr = *(const LAS v4u*)(L + L_WO + ((32 * w + 8 * rgp + k) * 14 + cc) * 4);
#pragma unroll
        for (int e = 0; e < 4; ++e) wf[k][e] = f32x2{bflo(wr[e]), bfhi(wr[e])};
    }
#pragma unroll
    for (int k = 0; k < 8; ++k) asm volatile("" : "+v"(wf[k][0]), "+v"(wf[k][1]), "+v"(wf[k][2]), "+v"(wf[k][3]));

    {
        if (w != 0) { for (unsigned spins = 0; *(volatile LAS unsigned*)(L + L_FLAG + 1) == 0u && spins < (SPIN_CAP << 4); ++spins) __builtin_amdgcn_s_sleep(1); }
        asm volatile("" ::: "memory");
        v4u qB[1][4];
#pragma unroll
        for (int jj = 0; jj < 4; ++jj) qB[0][jj] = *(const LAS v4u*)((const LAS unsigned char*)(L + L_Q) + 1024 + 16 * ((lane & 7) + 8 * jj));
        GAS unsigned* ctr = (GAS unsigned*)(p.ws + WS_CTR) + hB * 64;
        asm volatile("" : "+v"(ctr));
        State<1> s; state_init(s);
        const rsrc_t rnull = rsrc(p.k[1], 0);
        unsigned on = baseB + STRIDE;
        int n = 1;
        bool have = true, onhalf = false, curhalf = false, more;
        do {
            const rsrc_t rk = have ? rkB : rnull, rv = have ? rvB : rnull;
            const rsrc_t rk2 = (have && !onhalf) ? rkB : rnull, rv2 = (have && !onhalf) ? rvB : rnull;
            SB();
            float x0[1] = {0.f}, x1[1] = {0.f}, p0[1], p1[1];
#pragma unroll
            for (int j = 0; j < 8; ++j) {
                if (j < 4) { qk_piece<1>(RK[j], qB, j & 3, x0); PIN(x0[0]); } else { qk_piece<1>(RK[j], qB, j & 3, x1); PIN(x1[0]); }
                RK[j] = TILE_LD(j < 4 ? rk : rk2, on, j);
                SB();
            }
            softmax_step<1>(x0, x1, s, p0, p1, curhalf);
            SB();
            unsigned tk = 0xffffffffu;
            if (n + 1 >= NSB && have) {
                if (n + 1 == NSB && w != 0) { for (unsigned spins = 0; *(volatile LAS unsigned*)(L + L_FLAG) == 0u && spins < (SPIN_CAP << 4); ++spins) __builtin_amdgcn_s_sleep(1); }
                if (lane == 0) tk = __hip_atomic_fetch_add(ctr, 1u, __ATOMIC_RELAXED, __HIP_MEMORY_SCOPE_AGENT);
            }
            SB();
#pragma unroll
            for (int j = 0; j < 8; ++j) {
                if (j < 4) pv_piece<1>(RV[j], p0, j & 3, s); else pv_piece<1>(RV[j], p1, j & 3, s);
                PIN4(s.acc[0], (j & 3) * 4);
                RV[j] = TILE_LD(j < 4 ? rv : rv2, on, j);
                SB();
            }
            more = have;
            curhalf = onhalf;
            ++n;
            if (n < NSB) on += STRIDE;
            else {
                const unsigned tt = __builtin_amdgcn_readfirstlane(tk);
                have = tt < (unsigned)NTICK; onhalf = tt >= (unsigned)NFULL;
                on = headB + POOL0 * TILE_B + (onhalf ? NFULL * TILE_B + (tt - NFULL) * (TILE_B / 2) : tt * TILE_B);
            }
        } while (more);
        flush_state<1>(s, L + L_REC + (w * 3 + 2) * REC, lane);
    }
    BARRIER();

    if (w < 3) {
        float M = M0;
#pragma unroll
        for (int ww = 0; ww < WAVES; ++ww) M = fmaxf(M, L[L_REC + (ww * 3 + w) * REC + 256]);
        f32x4 a = {0.f, 0.f, 0.f, 0.f}; float Ls = 0.f;
#pragma unroll
        for (int ww = 0; ww < WAVES; ++ww) {
            const LAS float* rec = L + L_REC + (ww * 3 + w) * REC;
            const float wt = ex2(rec[256] - M);
            a += *(const LAS f32x4*)(rec + lane * 4) * wt; Ls = fmaf(rec[257], wt, Ls);
        }
        const int dst = w < 2 ? WS_PA + ((2 * gA + w) * 32 + iA) * 1024 : WS_PB + (hB * 16 + jB) * 1024;
        const int mlo = w < 2 ? WS_MLA + ((2 * gA + w) * 32 + iA) * 16 : WS_MLB + (hB * 16 + jB) * 16;
        st128c(__builtin_bit_cast(v4u, a), rws, dst + lane * 16);
        if (lane == 0) st64c(v2u{asu(M), asu(Ls)}, rws, mlo);
    }

    {
        const int S8 = cpr == 0 ? 4 : 2;
        const int sg = lane >> 3, cl = lane & 7;
        const int mlb = (cpr == 0 ? WS_MLA : WS_MLB) + (chq * S8 * 8 + sg) * 16;
        const int pb = (cpr == 0 ? WS_PA : WS_PB) + (chq * S8 * 8 + sg) * 1024 + chf * 512 + (8 * w + cl) * 16;
        v2u ml[4]; v4u d[4];
        for (unsigned spins = 0;; ++spins) {
            unsigned mx = 0;
#pragma unroll
            for (int i = 0; i < 4; ++i) ml[i] = ld64c(rws, mlb + (i < S8 ? i : 0) * 128);
#pragma unroll
            for (int i = 0; i < 4; ++i) mx = max(mx, max(ml[i].x, ml[i].y));
            if (all_lanes(mx != INVALID) || spins > SPIN_CAP) break;
            __builtin_amdgcn_s_sleep(2);
        }
        for (unsigned spins = 0;; ++spins) {
            unsigned mx = 0;
#pragma unroll
            for (int i = 0; i < 4; ++i) d[i] = ld128c(rws, pb + (i < S8 ? i : 0) * 8192);
#pragma unroll
            for (int i = 0; i < 4; ++i) mx = max(mx, umax4(d[i]));
            if (all_lanes(mx != INVALID) || spins > SPIN_CAP) break;
            __builtin_amdgcn_s_sleep(1);
        }
        float M = M0;
#pragma unroll
        for (int i = 0; i < 4; ++i) if (i < S8) M = fmaxf(M, asf(ml[i].x));
        M = xmax_groups(M);
        float Lt = 0.f; f32x4 a = {0.f, 0.f, 0.f, 0.f};
#pragma unroll
        for (int i = 0; i < 4; ++i) if (i < S8) { const float wt = ex2(asf(ml[i].x) - M); Lt = fmaf(wt, asf(ml[i].y), Lt); a += __builtin_bit_cast(f32x4, d[i]) * wt; }
        const float inv = 1.0f / xsum_groups(Lt);
        f32x4 o4;
#pragma unroll
        for (int e = 0; e < 4; ++e) o4[e] = bflo(cvtpk(xsum_groups(a[e]) * inv, 0.f));
        if (lane < 8) *(LAS f32x4*)(L + L_OV + 32 * w + 4 * lane) = o4;
        asm volatile("s_waitcnt lgkmcnt(0)" ::: "memory");
        const f32x4 oa = *(const LAS f32x4*)(L + L_OV + 32 * w + 8 * rgp), ob = *(const LAS f32x4*)(L + L_OV + 32 * w + 8 * rgp + 4);
        f32x2 y2[4] = {f32x2{0.f, 0.f}, f32x2{0.f, 0.f}, f32x2{0.f, 0.f}, f32x2{0.f, 0.f}};
#pragma unroll
        for (int k = 0; k < 8; ++k) {
            const float o = k < 4 ? oa[k & 3] : ob[k & 3];
#pragma unroll
            for (int e = 0; e < 4; ++e) y2[e] = __builtin_elementwise_fma(f32x2{o, o}, wf[k][e], y2[e]);
        }
        float b4[4], c2[2];
#pragma unroll
        for (int e = 0; e < 4; ++e) b4[e] = swap16_add(y2[e >> 1][e & 1], y2[2 + (e >> 1)][e & 1]);
#pragma unroll
        for (int e = 0; e < 2; ++e) c2[e] = swap32_add(b4[e], b4[e + 2]);
        if ((lane & 15) < 14) *(LAS f32x2*)(L + L_RED + w * 128 + cc * 8 + 2 * (lane >> 5) + 4 * ((lane >> 4) & 1)) = f32x2{c2[0], c2[1]};
    }
    BARRIER();
    if (t < 28) {
        const f32x4 v = (*(const LAS f32x4*)(L + L_RED + t * 4) + *(const LAS f32x4*)(L + L_RED + 128 + t * 4)) +
                        (*(const LAS f32x4*)(L + L_RED + 256 + t * 4) + *(const LAS f32x4*)(L + L_RED + 384 + t * 4));
        st128c(__builtin_bit_cast(v4u, v), rws, ybox + t * 16);
    }

    if ((it & 7) == 0 && t < 224) {
        const int r = it >> 3, fpr = r >> 4, fcb = (r >> 2) & 3, sub = r & 3;
        const int c = t >> 5, s = t & 31;
        const int off = WS_Y + (((fpr * 4 + fcb) * 32 + s) * 112 + sub * 28 + c * 4) * 4;
        v4u d;
        for (unsigned spins = 0;; ++spins) {
            d = ld128c(rws, off);
            if (all_lanes(umax4(d) != INVALID) || spins > SPIN_CAP) break;
            __builtin_amdgcn_s_sleep(1);
        }
        float v[4] = {asf(d.x), asf(d.y), asf(d.z), asf(d.w)};
#pragma unroll
        for (int e = 0; e < 4; ++e) { float z = v[e]; z += dpp<XOR1>(z); z += dpp<XOR2>(z); z += dpp<HMIRROR>(z); z += dpp<ROR8>(z); v[e] = swap16_add(z, z); }
        if (s == 0) st64c(v2u{cvtpk(v[0], v[1]), cvtpk(v[2], v[3])}, rsrc(p.out, 2 * DM * 2), (fpr * DM + fcb * 112 + sub * 28 + c * 4) * 2);
    }
}
}

extern "C" void kernel_launch(void* const* d_in, const int* in_sizes, int n_in, void* d_out, int out_size, void* d_ws, size_t ws_size,
                              hipStream_t stream) {
    Params p;
    p.x[0] = (const unsigned short*)d_in[0]; p.k[0] = (const unsigned char*)d_in[1]; p.v[0] = (const unsigned char*)d_in[2];
    p.wq[0] = (const unsigned short*)d_in[3]; p.wo[0] = (const unsigned short*)d_in[4];
    p.x[1] = (const unsigned short*)d_in[5]; p.k[1] = (const unsigned char*)d_in[6]; p.v[1] = (const unsigned char*)d_in[7];
    p.wq[1] = (const unsigned short*)d_in[8]; p.wo[1] = (const unsigned short*)d_in[9];
    p.out = (unsigned short*)d_out; p.ws = (unsigned char*)d_ws;
    k_fused<<<NWG, NTHR, 0, stream>>>(p);
}
```
